```python
import torch

def _softmax(x, axis=-1):
    return torch.softmax(x, dim=axis)
def _gelu(x):
    return torch.nn.functional.gelu(x, approximate="tanh")
def _silu(x):
    return torch.nn.functional.silu(x)
def _sigmoid(x):
    return torch.sigmoid(x)
def _layernorm(x, axis=-1, eps=1e-5):
    m = x.mean(dim=axis, keepdim=True)
    v = x.var(dim=axis, keepdim=True, unbiased=False)
    return (x - m) / torch.sqrt(v + eps)
def _scatter_add(tgt, idx, src, axis=0):
    return tgt.clone().index_add_(axis, idx, src)
def _segment_sum(src, seg, n):
    out = torch.zeros((n,) + src.shape[1:], dtype=src.dtype)
    return out.index_add_(0, seg, src)
def _pad(x, pw):
    flat = tuple(v for pair in reversed(pw) for v in pair)
    return torch.nn.functional.pad(x, flat)

kernel_name = 'recurrent_scan__sliding_block_cached__glu_gated_geglu__decode'

def setup_inputs():

    _w = (lambda a: (lambda t: t.to(torch.bfloat16) if t.is_floating_point() else t)(torch.as_tensor(a)))
    def _A_setup():
        B, Sq, D = 1, 1, 512
        Hq, Hkv, Sk, Dh = 32, 8, 65536, 128
        assert Hq % Hkv == 0, (Hq, Hkv)
        return {
            "x": _w((lambda *s: torch.randn(*s, dtype=torch.bfloat16) * 0.4)(B, Sq, D)),
            "k_cache": _w((lambda *s: torch.randn(*s, dtype=torch.bfloat16) * 1.6)(B, Hkv, Sk, Dh)),
            "v_cache": _w((lambda *s: torch.randn(*s, dtype=torch.bfloat16) * 1.6)(B, Hkv, Sk, Dh)),
            "wq": _w((lambda *s: torch.randn(*s, dtype=torch.bfloat16) * 1.6)(D, Hq * Dh) * (D ** -0.5)),
            "wo": _w((lambda *s: torch.randn(*s, dtype=torch.bfloat16) * 1.6)(Hq * Dh, D) * ((Hq * Dh) ** -0.5)),
        }
    _A_in = _A_setup()

    def _B_setup():
        B, S, Dm, Df = 1, 1, 512, 1792
        return {
            "x": _w((lambda *s: torch.randn(*s, dtype=torch.bfloat16) * 0.4)(B, S, Dm)),
            "w_gate": _w((lambda *s: torch.randn(*s, dtype=torch.bfloat16) * 1.6)(Dm, Df) * (Dm ** -0.5)),
            "w_up": _w((lambda *s: torch.randn(*s, dtype=torch.bfloat16) * 1.6)(Dm, Df) * (Dm ** -0.5)),
            "w_down": _w((lambda *s: torch.randn(*s, dtype=torch.bfloat16) * 1.6)(Df, Dm) * (Df ** -0.5)),
        }
    _B_in = _B_setup()

    return {
        "A_x": _A_in["x"],
        "A_k_cache": _A_in["k_cache"],
        "A_v_cache": _A_in["v_cache"],
        "A_wq": _A_in["wq"],
        "A_wo": _A_in["wo"],
        "B_x": _B_in["x"],
        "B_w_gate": _B_in["w_gate"],
        "B_w_up": _B_in["w_up"],
        "B_w_down": _B_in["w_down"],
    }

def reference(A_x, A_k_cache, A_v_cache, A_wq, A_wo, B_x, B_w_gate, B_w_up, B_w_down):
    def _A_ref(x, k_cache, v_cache, wq, wo):
        B, Sq, D = x.shape
        Hq, Dh = 32, 128
        Hkv, Sk = k_cache.shape[1], k_cache.shape[2]
        W = min(4096, max(256, Sk // 4))
        rep = Hq // Hkv
        q = (lambda x, *d: x.permute(*d))((x @ wq).reshape(B, Sq, Hq, Dh), 0, 2, 1, 3)
        k = (lambda x, n, axis: torch.repeat_interleave(x, n, dim=axis))(k_cache[:, :, Sk - W:, :], rep, axis=1)
        v = (lambda x, n, axis: torch.repeat_interleave(x, n, dim=axis))(v_cache[:, :, Sk - W:, :], rep, axis=1)
        scores = torch.einsum("bhqd,bhkd->bhqk", q, k) * (Dh ** -0.5)
        p = _softmax(scores, axis=-1)
        o = torch.einsum("bhqk,bhkd->bhqd", p, v)
        return (lambda x, *d: x.permute(*d))(o, 0, 2, 1, 3).reshape(B, Sq, Hq * Dh) @ wo

    def _B_ref(x, w_gate, w_up, w_down):
        g = torch.einsum("bsd,df->bsf", x, w_gate)
        u = torch.einsum("bsd,df->bsf", x, w_up)
        h = _gelu(g) * u
        return torch.einsum("bsf,fd->bsd", h, w_down)

    x = A_x
    xn = _layernorm(x, axis=-1)
    s = _A_ref(xn, A_k_cache, A_v_cache, A_wq, A_wo)
    g = _silu(_B_ref(xn, B_w_gate, B_w_up, B_w_down))
    return x + s * g
```

```cpp
#include <hip/hip_runtime.h>
#include <hip/hip_bf16.h>
#include <stdint.h>

typedef unsigned short u16;
typedef unsigned u32x4 __attribute__((ext_vector_type(4)));

constexpr int D = 512, HQ = 32, HKV = 8, SK = 65536, DH = 128, W = 4096, DF = 1792;
constexpr int NSPLIT = 32, KPS = W / NSPLIT;

__device__ __forceinline__ float bf2f(u16 v) { return __uint_as_float(((unsigned)v) << 16); }
__device__ __forceinline__ float rbf(float f) {
  return __bfloat162float(__float2bfloat16(f));
}
__device__ __forceinline__ float lo(unsigned w) { return __uint_as_float(w << 16); }
__device__ __forceinline__ float hi(unsigned w) { return __uint_as_float(w & 0xffff0000u); }

__device__ void layernorm_block(const u16* x, float* xn, float* red) {
  int t = threadIdx.x;
  float a = bf2f(x[2 * t]), b = bf2f(x[2 * t + 1]);
  float s = a + b;
  for (int o = 32; o; o >>= 1) s += __shfl_xor(s, o);
  if ((t & 63) == 0) red[t >> 6] = s;
  __syncthreads();
  float mean = (red[0] + red[1] + red[2] + red[3]) * (1.f / D);
  __syncthreads();
  float da = a - mean, db = b - mean;
  float v = da * da + db * db;
  for (int o = 32; o; o >>= 1) v += __shfl_xor(v, o);
  if ((t & 63) == 0) red[t >> 6] = v;
  __syncthreads();
  float var = (red[0] + red[1] + red[2] + red[3]) * (1.f / D);
  float mb = rbf(mean), vb = rbf(var);
  float den = rbf(sqrtf(rbf(vb + 1e-5f)));
  xn[2 * t] = rbf(rbf(a - mb) / den);
  xn[2 * t + 1] = rbf(rbf(b - mb) / den);
  __syncthreads();
}

__device__ __forceinline__ float gelu_tanh(float x) {
  float u = 0.7978845608028654f * (x + 0.044715f * x * x * x);
  return 0.5f * x * (1.f + tanhf(u));
}

__global__ void __launch_bounds__(256) k1(const u16* x, const u16* wq, const u16* wg, const u16* wu, float* q, float* h) {
  __shared__ float xn[D];
  __shared__ float red[4];
  __shared__ float part[2][32][64];
  layernorm_block(x, xn, red);
  int t = threadIdx.x, cg = t & 7, rg = t >> 3;
  if (blockIdx.x < 64) {
    int c0 = blockIdx.x * 64 + cg * 8;
    float acc[8] = {0};
    for (int r = rg; r < D; r += 32) {
      u32x4 w = *(const u32x4*)(wq + (size_t)r * (HQ * DH) + c0);
      float xv = xn[r];
      for (int k = 0; k < 4; ++k) { acc[2 * k] += xv * lo(w[k]); acc[2 * k + 1] += xv * hi(w[k]); }
    }
    for (int k = 0; k < 8; ++k) part[0][rg][cg * 8 + k] = acc[k];
    __syncthreads();
    if (t < 64) { float s = 0; for (int r = 0; r < 32; ++r) s += part[0][r][t]; q[blockIdx.x * 64 + t] = rbf(s); }
  } else {
    int b = blockIdx.x - 64;
    int c0 = b * 64 + cg * 8;
    float ag[8] = {0}, au[8] = {0};
    for (int r = rg; r < D; r += 32) {
      u32x4 w = *(const u32x4*)(wg + (size_t)r * DF + c0);
      u32x4 w2 = *(const u32x4*)(wu + (size_t)r * DF + c0);
      float xv = xn[r];
      for (int k = 0; k < 4; ++k) { ag[2 * k] += xv * lo(w[k]); ag[2 * k + 1] += xv * hi(w[k]); au[2 * k] += xv * lo(w2[k]); au[2 * k + 1] += xv * hi(w2[k]); }
    }
    for (int k = 0; k < 8; ++k) { part[0][rg][cg * 8 + k] = ag[k]; part[1][rg][cg * 8 + k] = au[k]; }
    __syncthreads();
    if (t < 64) {
      float g = 0, u = 0;
      for (int r = 0; r < 32; ++r) { g += part[0][r][t]; u += part[1][r][t]; }
      g = rbf(g); u = rbf(u);
      h[b * 64 + t] = rbf(rbf(gelu_tanh(g)) * u);
    }
  }
}

__global__ void __launch_bounds__(256) k2(const u16* kc, const u16* vc, const float* q, const float* h, const u16* wd,
                                         float* pm, float* pl, float* po, float* pd) {
  __shared__ float qs[4 * DH];
  __shared__ float sc[4][KPS];
  __shared__ float red[4][4];
  __shared__ float ored[16][4 * DH];
  int t = threadIdx.x, lane = t & 63, wave = t >> 6;
  if (blockIdx.x >= 256) {
    int b = blockIdx.x - 256;
    int cg = t & 63, rg = t >> 6;
    float acc[8] = {0};
    for (int r = rg; r < 64; r += 4) {
      int row = b * 64 + r;
      u32x4 w = *(const u32x4*)(wd + (size_t)row * D + cg * 8);
      float hv = h[row];
      for (int k = 0; k < 4; ++k) { acc[2 * k] += hv * lo(w[k]); acc[2 * k + 1] += hv * hi(w[k]); }
    }
    for (int k = 0; k < 8; ++k) ored[rg][cg * 8 + k] = acc[k];
    __syncthreads();
    for (int c = t; c < D; c += 256) pd[b * D + c] = ored[0][c] + ored[1][c] + ored[2][c] + ored[3][c];
    return;
  }
  int kvh = blockIdx.x & 7, sp = blockIdx.x >> 3;
  for (int i = t; i < 4 * DH; i += 256) qs[i] = q[kvh * 4 * DH + i];
  __syncthreads();
  const u16* kb = kc + ((size_t)kvh * SK + (SK - W) + sp * KPS) * DH;
  const u16* vb = vc + ((size_t)kvh * SK + (SK - W) + sp * KPS) * DH;
  int c = t & 15, r = t >> 4;
  for (int i = 0; i < KPS / 16; ++i) {
    int key = i * 16 + r;
    u32x4 w = *(const u32x4*)(kb + (size_t)key * DH + c * 8);
    for (int g = 0; g < 4; ++g) {
      float s = 0;
      for (int k = 0; k < 4; ++k) { s += lo(w[k]) * qs[g * DH + c * 8 + 2 * k] + hi(w[k]) * qs[g * DH + c * 8 + 2 * k + 1]; }
      for (int o = 8; o; o >>= 1) s += __shfl_xor(s, o);
      if (c == 0) sc[g][key] = rbf(rbf(s) * 0.08838834764831845f);
    }
  }
  __syncthreads();
  {
    int g = wave;
    float a = sc[g][lane], b2 = sc[g][lane + 64];
    float m = fmaxf(a, b2);
    for (int o = 32; o; o >>= 1) m = fmaxf(m, __shfl_xor(m, o));
    float ea = __expf(a - m), eb = __expf(b2 - m);
    float l = ea + eb;
    for (int o = 32; o; o >>= 1) l += __shfl_xor(l, o);
    sc[g][lane] = ea; sc[g][lane + 64] = eb;
    if (lane == 0) { int hq = kvh * 4 + g; pm[hq * NSPLIT + sp] = m; pl[hq * NSPLIT + sp] = l; }
  }
  __syncthreads();
  float acc[4][8] = {{0}};
  for (int i = 0; i < KPS / 16; ++i) {
    int key = i * 16 + r;
    u32x4 w = *(const u32x4*)(vb + (size_t)key * DH + c * 8);
    for (int g = 0; g < 4; ++g) {
      float p = sc[g][key];
      for (int k = 0; k < 4; ++k) { acc[g][2 * k] += p * lo(w[k]); acc[g][2 * k + 1] += p * hi(w[k]); }
    }
  }
  for (int g = 0; g < 4; ++g) for (int k = 0; k < 8; ++k) ored[r][g * DH + c * 8 + k] = acc[g][k];
  __syncthreads();
  for (int i = t; i < 4 * DH; i += 256) {
    float s = 0;
    for (int rr = 0; rr < 16; ++rr) s += ored[rr][i];
    int g = i >> 7, d = i & 127;
    po[((size_t)(kvh * 4 + g) * NSPLIT + sp) * DH + d] = s;
  }
}

__global__ void __launch_bounds__(256) k3(const float* pm, const float* pl, const float* po, const u16* wo, float* ps) {
  __shared__ float o[DH];
  __shared__ float part[32][64];
  int t = threadIdx.x;
  int hq = blockIdx.x >> 3, cgp = blockIdx.x & 7;
  if (t < DH) {
    float M = -INFINITY;
    for (int s = 0; s < NSPLIT; ++s) M = fmaxf(M, pm[hq * NSPLIT + s]);
    float L = 0, a = 0;
    for (int s = 0; s < NSPLIT; ++s) {
      float w = __expf(pm[hq * NSPLIT + s] - M);
      L += w * pl[hq * NSPLIT + s];
      a += w * po[((size_t)hq * NSPLIT + s) * DH + t];
    }
    o[t] = rbf(a / L);
  }
  __syncthreads();
  int cg = t & 7, rg = t >> 3;
  float acc[8] = {0};
  for (int r = rg; r < DH; r += 32) {
    u32x4 w = *(const u32x4*)(wo + (size_t)(hq * DH + r) * D + cgp * 64 + cg * 8);
    float ov = o[r];
    for (int k = 0; k < 4; ++k) { acc[2 * k] += ov * lo(w[k]); acc[2 * k + 1] += ov * hi(w[k]); }
  }
  for (int k = 0; k < 8; ++k) part[rg][cg * 8 + k] = acc[k];
  __syncthreads();
  if (t < 64) { float s = 0; for (int r = 0; r < 32; ++r) s += part[r][t]; ps[hq * D + cgp * 64 + t] = s; }
}

__global__ void __launch_bounds__(256) k4(const u16* x, const float* ps, const float* pd, __hip_bfloat16* out) {
  int c = blockIdx.x * 256 + threadIdx.x;
  float s = 0, d = 0;
  for (int h = 0; h < HQ; ++h) s += ps[h * D + c];
  for (int b = 0; b < 28; ++b) d += pd[b * D + c];
  s = rbf(s); d = rbf(d);
  float g = rbf(d / (1.f + __expf(-d)));
  out[c] = __float2bfloat16(bf2f(x[c]) + rbf(s * g));
}

extern "C" void kernel_launch(void* const* d_in, const int* in_sizes, int n_in, void* d_out, int out_size, void* d_ws,
                              size_t ws_size, hipStream_t stream) {
  const u16* x = (const u16*)d_in[0];
  const u16* kc = (const u16*)d_in[1];
  const u16* vc = (const u16*)d_in[2];
  const u16* wq = (const u16*)d_in[3];
  const u16* wo = (const u16*)d_in[4];
  const u16* wg = (const u16*)d_in[6];
  const u16* wu = (const u16*)d_in[7];
  const u16* wd = (const u16*)d_in[8];
  float* ws = (float*)d_ws;
  float* q = ws;
  float* h = q + 4096;
  float* pm = h + 2048;
  float* pl = pm + 1024;
  float* po = pl + 1024;
  float* pd = po + 32 * 32 * 128;
  float* ps = pd + 28 * 512;
  k1<<<64 + 28, 256, 0, stream>>>(x, wq, wg, wu, q, h);
  k2<<<256 + 28, 256, 0, stream>>>(kc, vc, q, h, wd, pm, pl, po, pd);
  k3<<<256, 256, 0, stream>>>(pm, pl, po, wo, ps);
  k4<<<2, 256, 0, stream>>>(x, ps, pd, (__hip_bfloat16*)d_out);
}
```

```cpp
#include <hip/hip_runtime.h>
#include <hip/hip_bf16.h>
#include <stdint.h>
#include <type_traits>

typedef unsigned short u16;
typedef unsigned long long u64;
typedef unsigned u32x4 __attribute__((ext_vector_type(4)));
typedef unsigned u32x2 __attribute__((ext_vector_type(2)));
typedef float f32x4 __attribute__((ext_vector_type(4)));
typedef float f32x2 __attribute__((ext_vector_type(2)));
typedef short s16x4 __attribute__((ext_vector_type(4)));
typedef short s16x8 __attribute__((ext_vector_type(8)));
typedef __bf16 bf16x8 __attribute__((ext_vector_type(8)));
typedef __bf16 bf16x2 __attribute__((ext_vector_type(2)));
#define LAS __attribute__((address_space(3)))
#define GAS __attribute__((address_space(1)))

#define THR 10
#define STAMPS 0
#define CODE_KB 0
#define LSLP 0
#define DBG 0
#define WS_OFF(s) 0
#define SLP 2

constexpr int SK = 65536, WIN = 4096, DF = 1792;
constexpr int L_WQ = 0, L_K = 16384, L_GU = 49152, L_V = 65536, L_WO = 98304, L_DN = 114688, L_MISC = 122880;
constexpr int L_XN = L_MISC, L_QA = L_XN + 1024, L_PB = L_QA + 1024, L_WS = L_PB + 1280, L_OA = L_WS + 128, L_HA = L_OA + 64,
              L_FL = L_HA + 64, L_END = L_FL + 128;
constexpr int F_CNT = L_FL, F_PF = L_FL + 16, F_POF = L_FL + 32, F_O3F = L_FL + 48, F_OAF = L_FL + 64, F_HAF = L_FL + 68;
constexpr int W_E = 0, W_QG = 4096, W_HG = W_QG + 65536, W_ML = W_HG + 65536, W_PO = W_ML + 16384 + 4096 * 3,
              W_O3 = W_PO + (1 << 20), W_END = W_O3 + (1 << 20);
constexpr int W_ST = 8 << 20;
constexpr int C_WQ = 8, C_K = 24, C_GU = 32, C_V0 = 36, C_WO = 56, C_DN = 60, C_ALL = 60;
constexpr unsigned SPIN_LDS = 1u << 22, SPIN_GLB = 1u << 17;
constexpr float LOG2E = 1.4426950408889634f;
constexpr int OOB = 0x7ffffff0;

__device__ __forceinline__ float bf_lo(unsigned w) { return __uint_as_float(w << 16); }
__device__ __forceinline__ float bf_hi(unsigned w) { return __uint_as_float(w & 0xffff0000u); }
__device__ __forceinline__ unsigned pack_bf(float lo, float hi) {
  f32x2 v = {lo, hi};
  return __builtin_bit_cast(unsigned, __builtin_convertvector(v, bf16x2));
}
__device__ __forceinline__ float rbf(float f) { return bf_lo(pack_bf(f, f)); }
__device__ __forceinline__ float rcp(float f) { return __builtin_amdgcn_rcpf(f); }
__device__ __forceinline__ float vmax(float a, float b) { float r; asm("v_max_f32 %0, %1, %2" : "=v"(r) : "v"(a), "v"(b)); return r; }
template <int CTRL> __device__ __forceinline__ float dpp(float x) {
  return __builtin_bit_cast(float, __builtin_amdgcn_mov_dpp(__builtin_bit_cast(int, x), CTRL, 0xf, 0xf, true));
}
constexpr int XOR1 = 0xB1, XOR2 = 0x4E, ROR4 = 0x124, ROR8 = 0x128;
__device__ __forceinline__ float xrow_sum(float x) {
  auto s = __builtin_amdgcn_permlane16_swap(__float_as_uint(x), __float_as_uint(x), false, false);
  x = __uint_as_float(s[0]) + __uint_as_float(s[1]);
  auto t = __builtin_amdgcn_permlane32_swap(__float_as_uint(x), __float_as_uint(x), false, false);
  return __uint_as_float(t[0]) + __uint_as_float(t[1]);
}
__device__ __forceinline__ float xrow_max(float x) {
  auto s = __builtin_amdgcn_permlane16_swap(__float_as_uint(x), __float_as_uint(x), false, false);
  x = fmaxf(__uint_as_float(s[0]), __uint_as_float(s[1]));
  auto t = __builtin_amdgcn_permlane32_swap(__float_as_uint(x), __float_as_uint(x), false, false);
  return fmaxf(__uint_as_float(t[0]), __uint_as_float(t[1]));
}
__device__ __forceinline__ float wave_sum(float x) {
  x += dpp<XOR1>(x); x += dpp<XOR2>(x); x += dpp<ROR4>(x); x += dpp<ROR8>(x);
  return xrow_sum(x);
}
__device__ __forceinline__ float wave_max(float x) {
  x = fmaxf(x, dpp<XOR1>(x)); x = fmaxf(x, dpp<XOR2>(x)); x = fmaxf(x, dpp<ROR4>(x)); x = fmaxf(x, dpp<ROR8>(x));
  return xrow_max(x);
}
__device__ __forceinline__ float sel4(int g, float a0, float a1, float a2, float a3) {
  const float lo = (g & 1) ? a1 : a0, hi = (g & 1) ? a3 : a2;
  return (g & 2) ? hi : lo;
}
__device__ __forceinline__ bool none(bool bad) { return __builtin_amdgcn_ballot_w64(bad) == 0ull; }
#define SCHED() __builtin_amdgcn_sched_barrier(0)
#define MEMBAR() asm volatile("" ::: "memory")
#define LDS_WAIT() asm volatile("s_waitcnt lgkmcnt(0)" ::: "memory")
__device__ __forceinline__ void lds_flag(int addr, unsigned v) {
  asm volatile("ds_write_b32 %0, %1" ::"v"(addr), "v"(v) : "memory");
}
__device__ __forceinline__ void wait_cnt(LAS char* L, unsigned thr) {
#pragma nounroll
  for (unsigned n = 0; n < SPIN_LDS; ++n) {
    u32x2 c = *(volatile LAS u32x2*)(L + F_CNT);
    if (__builtin_amdgcn_readfirstlane(min(c[0], c[1])) >= thr) break;
    if (LSLP) __builtin_amdgcn_s_sleep(LSLP);
  }
  MEMBAR();
}
__device__ __forceinline__ void wait_flag4(LAS char* L, int off) {
#pragma nounroll
  for (unsigned n = 0; n < SPIN_LDS; ++n) {
    u32x4 c = *(volatile LAS u32x4*)(L + off);
    if (__builtin_amdgcn_readfirstlane(c[0] & c[1] & c[2] & c[3])) break;
    if (LSLP) __builtin_amdgcn_s_sleep(LSLP);
  }
  MEMBAR();
}
__device__ __forceinline__ void wait_flag1(LAS char* L, int off) {
#pragma nounroll
  for (unsigned n = 0; n < SPIN_LDS; ++n) {
    unsigned c = *(volatile LAS unsigned*)(L + off);
    if (__builtin_amdgcn_readfirstlane(c)) break;
    if (LSLP) __builtin_amdgcn_s_sleep(LSLP);
  }
  MEMBAR();
}
__device__ __forceinline__ bf16x8 tr_pair(LAS char* L, int a0, int a1) {
  s16x4 t0 = __builtin_amdgcn_ds_read_tr16_b64_v4i16((LAS s16x4*)(L + a0));
  s16x4 t1 = __builtin_amdgcn_ds_read_tr16_b64_v4i16((LAS s16x4*)(L + a1));
  s16x8 b = {t0[0], t0[1], t0[2], t0[3], t1[0], t1[1], t1[2], t1[3]};
  return __builtin_bit_cast(bf16x8, b);
}
#define MFMA32(a, b, c) __builtin_amdgcn_mfma_f32_16x16x32_bf16((a), (b), (c), 0, 0, 0)
#define DMA(src, dst) __builtin_amdgcn_global_load_lds((const unsigned*)(src), (LAS unsigned*)(dst), 16, 0, 2)
#define SC1 16
#define LD4(off, soff) __builtin_bit_cast(u32x4, __builtin_amdgcn_raw_buffer_load_b128(wsr, (off), (soff), SC1))

template <int N, int SLEEP>
__device__ __forceinline__ void gather(__amdgpu_buffer_rsrc_t wsr, int off, int stride, unsigned tag, float (&lo)[N], float (&hi)[N]) {
  int o[N];
#pragma unroll
  for (int k = 0; k < N; ++k) { o[k] = off; lo[k] = 0.f; hi[k] = 0.f; }
#pragma nounroll
  for (unsigned n = 0; n < SPIN_GLB; ++n) {
    MEMBAR();
    u32x4 v[N];
#pragma unroll
    for (int k = 0; k < N; ++k) v[k] = LD4(o[k], k * stride);
    bool pend = false;
#pragma unroll
    for (int k = 0; k < N; ++k) {
      const bool ok = (v[k][1] == tag) & (v[k][3] == tag) & (o[k] != OOB);
      lo[k] = ok ? __uint_as_float(v[k][0]) : lo[k];
      hi[k] = ok ? __uint_as_float(v[k][2]) : hi[k];
      o[k] = ok ? OOB : o[k];
      pend |= o[k] != OOB;
    }
    if (none(pend)) break;
    if (SLEEP) __builtin_amdgcn_s_sleep(SLEEP);
  }
}

#define STAMP(k) do { } while (0)

template <int J> __device__ __forceinline__ void after_issue(int cnt_addr) {
  if constexpr (J > THR) {
    asm volatile("s_waitcnt vmcnt(%0)" ::"n"(THR) : "memory");
    constexpr int LANDED = J - THR;
    if constexpr (LANDED == C_WQ || LANDED == C_K || LANDED == C_GU || (LANDED >= C_V0 && LANDED <= 48 && (LANDED & 3) == 0) ||
                  LANDED == C_WO)
      lds_flag(cnt_addr, LANDED);
  }
}
template <int LANDED> __device__ __forceinline__ void drain_to(int cnt_addr) {
  if constexpr (LANDED > C_ALL - THR) {
    asm volatile("s_waitcnt vmcnt(%0)" ::"n"(C_ALL - LANDED) : "memory");
    lds_flag(cnt_addr, LANDED);
  }
}
template <int J0, int N, typename F> __device__ __forceinline__ void issue_run(F f) {
  if constexpr (N > 0) {
    f(std::integral_constant<int, J0>{});
    issue_run<J0 + 1, N - 1>(f);
  }
}

__global__ void __launch_bounds__(512, 2) __attribute__((amdgpu_waves_per_eu(2, 2)))
fused_decode(const u16* __restrict__ x, const u16* __restrict__ kc, const u16* __restrict__ vc, const u16* __restrict__ wq,
             const u16* __restrict__ wo, const u16* __restrict__ wg, const u16* __restrict__ wu, const u16* __restrict__ wd,
             u16* __restrict__ out, char* ws) {
  extern __shared__ __attribute__((aligned(16))) char lds_raw[];
  LAS char* L = (LAS char*)lds_raw;
  const int b = blockIdx.x, tid = threadIdx.x, lane = tid & 63;
  const int wv = __builtin_amdgcn_readfirstlane(tid >> 6);
  const int h = b & 7, i = b >> 3;
  const int g = lane >> 4, c = lane & 15;
  const int q_rh = i >> 4, q_col0 = 512 * h + 32 * (i & 15);
  const int gi = i < 28 ? i : i - 4;
  const int combo = (gi >> 1) * 8 + h;
  const int u_mat = (combo >> 1) & 1, u_rh = combo & 1, u_col0 = (combo >> 2) * 64 + (gi & 1) * 32;
  const int rg = h * 16 + (i >> 1), ch = i & 1;
  STAMP(0);

  if (wv >= 2) {
  if (wv == 7 && lane < 32) *(LAS unsigned*)(L + L_FL + lane * 4) = 0u;
  __builtin_amdgcn_s_setprio(2);
  const __amdgpu_buffer_rsrc_t wsr = __builtin_amdgcn_make_buffer_rsrc((void*)ws, 0, W_END, 0x00020000);
  const u32x4 xv = *(const GAS u32x4*)(x + lane * 8);
  const unsigned e_in = __hip_atomic_load((GAS unsigned*)(ws + W_E), __ATOMIC_RELAXED, __HIP_MEMORY_SCOPE_AGENT);
  SCHED();
  LDS_WAIT();
  __builtin_amdgcn_s_barrier();
  SCHED();

  if (wv < 6) {
    const int cw = wv - 2;
    {
      float f[8];
#pragma unroll
      for (int k = 0; k < 4; ++k) { f[2 * k] = bf_lo(xv[k]); f[2 * k + 1] = bf_hi(xv[k]); }
      float s = ((f[0] + f[1]) + (f[2] + f[3])) + ((f[4] + f[5]) + (f[6] + f[7]));
      const float mean = wave_sum(s) * (1.f / 512.f);
      float v0 = 0.f, v1 = 0.f;
#pragma unroll
      for (int k = 0; k < 8; k += 2) { const float d0 = f[k] - mean, d1 = f[k + 1] - mean; v0 = fmaf(d0, d0, v0); v1 = fmaf(d1, d1, v1); }
      const float var = wave_sum(v0 + v1) * (1.f / 512.f);
      const float mb = rbf(mean);
      const float den = rbf(__builtin_amdgcn_sqrtf(rbf(rbf(var) + 1e-5f)));
      const float inv = rcp(den);
      u32x4 xn;
#pragma unroll
      for (int k = 0; k < 4; ++k) {
        const unsigned d = pack_bf(f[2 * k] - mb, f[2 * k + 1] - mb);
        float a0 = bf_lo(d), a1 = bf_hi(d);
        float q0 = a0 * inv, q1 = a1 * inv;
        q0 = fmaf(fmaf(-q0, den, a0), inv, q0); q1 = fmaf(fmaf(-q1, den, a1), inv, q1);
        xn[k] = pack_bf(q0, q1);
      }
      *(LAS u32x4*)(L + L_XN + lane * 16) = xn;
    }
    const unsigned tag = __builtin_amdgcn_readfirstlane(e_in) * 1664525u + 1013904223u;
    STAMP(1);
    const int q4 = (lane & 15) >> 2, pp = lane & 3;
    const int p_tile = cw < 2 ? L_WQ : L_GU, p_rh = cw < 2 ? q_rh : u_rh;
    bf16x8 xa[8];
#pragma unroll
    for (int ks = 0; ks < 8; ++ks) xa[ks] = *(LAS bf16x8*)(L + L_XN + p_rh * 512 + g * 16 + ks * 64);
    auto proj = [&]() -> float {
      const int tb = p_tile + (8 * g + q4) * 64 + (((2 * (cw & 1) + (pp >> 1)) ^ ((g & 1) << 1)) * 16) + 8 * (pp & 1);
      bf16x8 wb[8];
#pragma unroll
      for (int ks = 0; ks < 8; ++ks) wb[ks] = tr_pair(L, tb + ks * 2048, tb + ks * 2048 + 256);
      SCHED();
      f32x4 a0 = {0.f, 0.f, 0.f, 0.f}, a1 = {0.f, 0.f, 0.f, 0.f};
#pragma unroll
      for (int ks = 0; ks < 8; ks += 2) { a0 = MFMA32(xa[ks], wb[ks], a0); a1 = MFMA32(xa[ks + 1], wb[ks + 1], a1); }
      return a0[0] + a1[0];
    };
    if (cw < 2) {
      wait_cnt(L, C_WQ);
      STAMP(2);
      const float qp = proj();
      if (lane < 16)
        __hip_atomic_store((GAS u64*)(ws + W_QG + (size_t)(q_rh * 4096 + q_col0 + 16 * cw + lane) * 8),
                           ((u64)tag << 32) | __float_as_uint(qp), __ATOMIC_RELAXED, __HIP_MEMORY_SCOPE_AGENT);
      STAMP(3);
    }
    wait_cnt(L, C_K);
    STAMP(4);
    {
      bf16x8 kb[8][4];
#pragma unroll
      for (int kt = 0; kt < 8; ++kt)
#pragma unroll
        for (int ks = 0; ks < 4; ++ks) kb[kt][ks] = *(LAS bf16x8*)(L + L_K + kt * 4096 + c * 256 + (((4 * ks + g) ^ c) * 16));
      SCHED();
      float qlo[2], qhi[2];
      gather<2, SLP>(wsr, W_QG + ((4 * h + cw) * 128 + 2 * lane) * 8, 32768, tag, qlo, qhi);
      STAMP(5);
      *(LAS unsigned*)(L + L_QA + cw * 256 + lane * 4) = pack_bf(qlo[0] + qlo[1], qhi[0] + qhi[1]);
      bf16x8 qa[4];
#pragma unroll
      for (int ks = 0; ks < 4; ++ks) qa[ks] = *(LAS bf16x8*)(L + L_QA + cw * 256 + ks * 64 + g * 16);
      SCHED();
      f32x4 sc[8];
#pragma unroll
      for (int kt = 0; kt < 8; ++kt) sc[kt] = MFMA32(qa[0], kb[kt][0], (f32x4{0.f, 0.f, 0.f, 0.f}));
#pragma unroll
      for (int ks = 1; ks < 4; ++ks)
#pragma unroll
        for (int kt = 0; kt < 8; ++kt) sc[kt] = MFMA32(qa[ks], kb[kt][ks], sc[kt]);
      float s0 = sel4(g, sc[0][0], sc[2][0], sc[4][0], sc[6][0]);
      float s1 = sel4(g, sc[1][0], sc[3][0], sc[5][0], sc[7][0]);
      unsigned r = pack_bf(s0, s1);
      r = pack_bf(bf_lo(r) * 0.08838834764831845f, bf_hi(r) * 0.08838834764831845f);
      s0 = bf_lo(r); s1 = bf_hi(r);
      const float m = wave_max(fmaxf(s0, s1));
      const float p0 = __builtin_amdgcn_exp2f((s0 - m) * LOG2E), p1 = __builtin_amdgcn_exp2f((s1 - m) * LOG2E);
      const unsigned pb = pack_bf(p0, p1);
      *(LAS u16*)(L + L_PB + cw * 320 + (32 * g + c) * 2) = (u16)pb;
      *(LAS u16*)(L + L_PB + cw * 320 + (32 * g + 16 + c) * 2) = (u16)(pb >> 16);
      const float l = wave_sum(p0 + p1);
      LDS_WAIT();
      lds_flag(F_PF + 4 * cw, 1);
      if (lane == 0) {
        const u32x4 ml = {__float_as_uint(m), tag, __float_as_uint(l), tag};
        __builtin_amdgcn_raw_buffer_store_b128(ml, wsr, W_ML + ((4 * h + cw) * 32 + i) * 16, 0, SC1);
      }
    }
    STAMP(6);
    if (cw >= 2) {
      wait_cnt(L, C_GU);
      const float up = proj();
      if (lane < 16 && i < 28)
        __hip_atomic_store((GAS u64*)(ws + W_HG + (size_t)((u_mat * 2 + u_rh) * DF + u_col0 + 16 * (cw - 2) + lane) * 8),
                           ((u64)tag << 32) | __float_as_uint(up), __ATOMIC_RELAXED, __HIP_MEMORY_SCOPE_AGENT);
      STAMP(7);
    }
    {
      wait_flag4(L, F_PF);
      bf16x8 pa[4];
#pragma unroll
      for (int ks = 0; ks < 4; ++ks) pa[ks] = *(LAS bf16x8*)(L + L_PB + (c >> 2) * 320 + ks * 64 + g * 16);
      const int f = q4 | ((g & 1) << 2);
      const int vb0 = L_V + (8 * g + q4) * 256 + (((2 * cw) ^ f) * 32) + pp * 8;
      const int vb1 = L_V + (8 * g + q4) * 256 + (((2 * cw + 1) ^ f) * 32) + pp * 8;
      f32x4 o0 = {0.f, 0.f, 0.f, 0.f}, o1 = {0.f, 0.f, 0.f, 0.f};
#pragma unroll
      for (int ks = 0; ks < 4; ++ks) {
        wait_cnt(L, C_V0 + 4 * ks);
        const bf16x8 b0 = tr_pair(L, vb0 + ks * 8192, vb0 + ks * 8192 + 1024);
        const bf16x8 b1 = tr_pair(L, vb1 + ks * 8192, vb1 + ks * 8192 + 1024);
        SCHED();
        o0 = MFMA32(pa[ks], b0, o0);
        o1 = MFMA32(pa[ks], b1, o1);
      }
      const u32x4 po = {__float_as_uint(o0[0]), tag, __float_as_uint(o1[0]), tag};
      __builtin_amdgcn_raw_buffer_store_b128(po, wsr, W_PO + (((4 * h + g) * 32 + i) * 128 + 32 * cw + 2 * c) * 8, 0, SC1);
      lds_flag(F_POF + 4 * cw, 1);
    }
    STAMP(9);
    {
      const int f = q4 | ((g & 1) << 2), fd = (4 * g + q4) & 7;
      bf16x8 wb[4];
      s16x4 db[4];
      wait_cnt(L, C_WO);
#pragma unroll
      for (int u = 0; u < 4; ++u) {
        const int a = L_WO + (8 * g + q4) * 512 + (((4 * cw + u) ^ f) * 32) + pp * 8;
        wb[u] = tr_pair(L, a, a + 2048);
      }
      wait_cnt(L, C_DN);
      STAMP(10);
#pragma unroll
      for (int u = 0; u < 4; ++u)
        db[u] = __builtin_amdgcn_ds_read_tr16_b64_v4i16((LAS s16x4*)(L + L_DN + (4 * g + q4) * 512 + (((4 * cw + u) ^ fd) * 32) + pp * 8));
      wait_flag1(L, F_HAF);
      const s16x4 ha = *(LAS s16x4*)(L + L_HA + g * 8);
      f32x4 dd[4], ss[4];
#pragma unroll
      for (int u = 0; u < 4; ++u) dd[u] = __builtin_amdgcn_mfma_f32_16x16x16bf16_1k(ha, db[u], f32x4{0.f, 0.f, 0.f, 0.f}, 0, 0, 0);
      const float dsel = sel4(g, dd[0][0], dd[1][0], dd[2][0], dd[3][0]);
      SCHED();
      wait_flag1(L, F_OAF);
      const bf16x8 oa = *(LAS bf16x8*)(L + L_OA + g * 16);
#pragma unroll
      for (int u = 0; u < 4; ++u) ss[u] = MFMA32(oa, wb[u], (f32x4{0.f, 0.f, 0.f, 0.f}));
      const float ssel = sel4(g, ss[0][0], ss[1][0], ss[2][0], ss[3][0]);
      const u32x4 o3 = {__float_as_uint(ssel), tag, __float_as_uint(dsel), tag};
      __builtin_amdgcn_raw_buffer_store_b128(o3, wsr, W_O3 + (rg * 512 + ch * 256 + cw * 64 + lane) * 16, 0, SC1);
      lds_flag(F_O3F + 4 * cw, 1);
    }
    STAMP(12);
    return;
  }

  if (wv == 6) {
    const int col0 = (h * 8 + (i >> 2)) * 8 + 2 * (i & 3);
    const unsigned xw = __builtin_amdgcn_readlane(xv[i & 3], col0 >> 3);
    const unsigned tag = __builtin_amdgcn_readfirstlane(e_in) * 1664525u + 1013904223u;
    const int r = rg >> 2, dc = rg & 3;
    wait_flag4(L, F_PF);
    wait_cnt(L, C_V0 + 4);
    STAMP(1);
    float w[8];
    {
      float m[1], l[1];
      gather<1, SLP>(wsr, W_ML + (r * 32 + (lane & 31)) * 16, 0, tag, m, l);
      const float M = wave_max(m[0]);
      const float e = __builtin_amdgcn_exp2f((m[0] - M) * LOG2E);
      const float Lsum = wave_sum(e * l[0]) * 0.5f;
      *(LAS float*)(L + L_WS + ((lane & 3) * 8 + ((lane & 31) >> 2)) * 4) = e * rcp(Lsum);
      const f32x4 w0 = *(LAS f32x4*)(L + L_WS + g * 32), w1 = *(LAS f32x4*)(L + L_WS + g * 32 + 16);
#pragma unroll
      for (int k = 0; k < 4; ++k) { w[k] = w0[k]; w[4 + k] = w1[k]; }
    }
    STAMP(2);
    wait_flag4(L, F_POF);
    STAMP(3);
    {
      float v0[8], v1[8];
      gather<8, 0>(wsr, W_PO + ((r * 32 + g) * 128 + 32 * dc + 2 * c) * 8, 4096, tag, v0, v1);
      STAMP(4);
      float a0 = 0.f, a1 = 0.f;
#pragma unroll
      for (int k = 0; k < 8; ++k) { a0 = fmaf(w[k], v0[k], a0); a1 = fmaf(w[k], v1[k], a1); }
      a0 = xrow_sum(a0); a1 = xrow_sum(a1);
      const unsigned ob = pack_bf(a0, a1);
      *(LAS u16*)(L + L_OA + c * 2) = (u16)ob;
      *(LAS u16*)(L + L_OA + 32 + c * 2) = (u16)(ob >> 16);
      LDS_WAIT();
      lds_flag(F_OAF, 1);
    }
    wait_flag4(L, F_O3F);
    STAMP(6);
    {
      float vs[4], vd[4];
      gather<4, 0>(wsr, W_O3 + ((lane >> 1) * 512 + col0 + (lane & 1)) * 16, 32 * 8192, tag, vs, vd);
      STAMP(7);
      float s = (vs[0] + vs[1]) + (vs[2] + vs[3]);
      float d = (vd[0] + vd[1]) + (vd[2] + vd[3]);
      s += dpp<XOR2>(s); d += dpp<XOR2>(d);
      s += dpp<ROR4>(s); d += dpp<ROR4>(d);
      s += dpp<ROR8>(s); d += dpp<ROR8>(d);
      s = xrow_sum(s); d = xrow_sum(d);
      const unsigned sd = pack_bf(s, d);
      s = bf_lo(sd); d = bf_hi(sd);
      const float gt = rbf(d * rcp(1.f + __builtin_amdgcn_exp2f(-d * LOG2E)));
      const float xe = (lane & 1) ? bf_hi(xw) : bf_lo(xw);
      const unsigned res = pack_bf(xe + rbf(s * gt), 0.f);
      if (lane < 2) *(GAS u16*)(out + col0 + lane) = (u16)res;
      if (b == 0 && lane == 0) __hip_atomic_store((GAS unsigned*)(ws + W_E), tag, __ATOMIC_RELAXED, __HIP_MEMORY_SCOPE_AGENT);
    }
    STAMP(8);
    return;
  }

  {
    const unsigned tag = __builtin_amdgcn_readfirstlane(e_in) * 1664525u + 1013904223u;
    wait_cnt(L, C_V0 + 8);
    STAMP(1);
    const GAS u64* src = (const GAS u64*)(ws + W_HG + (size_t)(g * DF + rg * 14 + min(c, 13)) * 8);
    u64 v;
#pragma nounroll
    for (unsigned n = 0; n < SPIN_GLB; ++n) {
      v = __hip_atomic_load(src, __ATOMIC_RELAXED, __HIP_MEMORY_SCOPE_AGENT);
      if (none((unsigned)(v >> 32) != tag)) break;
      __builtin_amdgcn_s_sleep(SLP);
    }
    STAMP(2);
    auto s = __builtin_amdgcn_permlane16_swap((unsigned)v, (unsigned)v, false, false);
    const float y = __uint_as_float(s[0]) + __uint_as_float(s[1]);
    auto t = __builtin_amdgcn_permlane32_swap(__float_as_uint(y), __float_as_uint(y), false, false);
    const unsigned gu = pack_bf(__uint_as_float(t[0]), __uint_as_float(t[1]));
    const float gg = bf_lo(gu), uu = bf_hi(gu);
    const float z = 0.7978845608028654f * (gg + 0.044715f * gg * gg * gg);
    const float ge = rbf(gg * rcp(1.f + __builtin_amdgcn_exp2f(-2.f * LOG2E * z)));
    const unsigned hb = c < 14 ? pack_bf(ge * uu, 0.f) : 0u;
    *(LAS u16*)(L + L_HA + c * 2) = (u16)hb;
    LDS_WAIT();
    lds_flag(F_HAF, 1);
    STAMP(3);
  }
  return;
  }

  {
    const int lw = wv;
    const int cnt_addr = F_CNT + 4 * lw;
    const size_t hoff = ((size_t)h * SK + (SK - WIN)) * 256;
    {
      const int row = lane >> 2, chunk = (lane & 3) ^ (((row >> 3) & 1) << 1);
      const char* src = (const char*)wq + (size_t)(q_rh * 256 + lw * 16) * 8192 + q_col0 * 2;
      const unsigned vo = row * 8192 + chunk * 16;
      issue_run<0, 8>([&](auto J) {
        constexpr int j = decltype(J)::value;
        DMA(src + (size_t)j * 32 * 8192 + vo, L + L_WQ + (2 * j + lw) * 1024);
        after_issue<j + 1>(cnt_addr);
      });
    }
    __builtin_amdgcn_s_barrier();
    {
      const char* src = (const char*)kc + hoff + lw * 1024;
      const unsigned vo0 = g * 256 + ((c ^ (4 * lw + g)) * 16), vo1 = g * 256 + ((c ^ (4 * (lw + 2) + g)) * 16) + 2048;
      issue_run<0, 16>([&](auto J) {
        constexpr int j = decltype(J)::value, blk = j >> 1;
        const char* s = src + (size_t)(blk * 32 + (i ^ ((blk & 1) << 1))) * 4096;
        DMA(s + ((j & 1) ? vo1 : vo0), L + L_K + (2 * j + lw) * 1024);
        after_issue<8 + j + 1>(cnt_addr);
      });
    }
    {
      const int row = lane >> 2, chunk = (lane & 3) ^ (((row >> 3) & 1) << 1);
      const char* src = (const char*)(u_mat ? wu : wg) + (size_t)(u_rh * 256 + lw * 16) * (DF * 2) + u_col0 * 2;
      const unsigned vo = row * (DF * 2) + chunk * 16;
      issue_run<0, 8>([&](auto J) {
        constexpr int j = decltype(J)::value;
        DMA(src + (size_t)j * 32 * (DF * 2) + vo, L + L_GU + (2 * j + lw) * 1024);
        after_issue<24 + j + 1>(cnt_addr);
      });
    }
    {
      const char* src = (const char*)vc + hoff + lw * 1024;
      const unsigned vo0 = g * 256 + ((c ^ (g << 1)) * 16), vo1 = g * 256 + ((c ^ ((g | 4) << 1)) * 16) + 2048;
      issue_run<0, 16>([&](auto J) {
        constexpr int j = decltype(J)::value, blk = j >> 1;
        const char* s = src + (size_t)(blk * 32 + (i ^ ((blk & 1) << 1))) * 4096;
        DMA(s + ((j & 1) ? vo1 : vo0), L + L_V + (2 * j + lw) * 1024);
        after_issue<32 + j + 1>(cnt_addr);
      });
    }
    {
      const int r2 = lane >> 5, pos = lane & 31;
      const char* src = (const char*)wo + (size_t)(rg * 32 + 2 * lw) * 1024 + ch * 512;
      const unsigned vo0 = r2 * 1024 + ((pos ^ ((2 * lw + r2) << 1)) * 16), vo1 = r2 * 1024 + ((pos ^ (((2 * lw + r2) | 4) << 1)) * 16);
      issue_run<0, 8>([&](auto J) {
        constexpr int j = decltype(J)::value;
        DMA(src + j * 4096 + (((j >> 1) & 1) ? vo1 : vo0), L + L_WO + (2 * j + lw) * 1024);
        after_issue<48 + j + 1>(cnt_addr);
      });
      const char* srd = (const char*)wd + (size_t)(rg * 14 + 2 * lw) * 1024 + ch * 512;
      issue_run<0, 4>([&](auto J) {
        constexpr int j = decltype(J)::value;
        const int back = (j == 3) ? lw * 2048 : 0;
        DMA(srd + j * 4096 - back + ((j & 1) ? vo1 : vo0), L + L_DN + (2 * j + lw) * 1024);
        after_issue<56 + j + 1>(cnt_addr);
      });
    }
    drain_to<C_WQ>(cnt_addr); drain_to<C_K>(cnt_addr); drain_to<C_GU>(cnt_addr);
    drain_to<36>(cnt_addr); drain_to<40>(cnt_addr); drain_to<44>(cnt_addr); drain_to<48>(cnt_addr);
    drain_to<C_WO>(cnt_addr); drain_to<C_DN>(cnt_addr);
    STAMP(2);
  }
}

extern "C" void kernel_launch(void* const* d_in, const int* in_sizes, int n_in, void* d_out, int out_size, void* d_ws,
                              size_t ws_size, hipStream_t stream) {
  (void)in_sizes; (void)n_in; (void)out_size; (void)ws_size;
  (void)hipFuncSetAttribute((const void*)fused_decode, hipFuncAttributeMaxDynamicSharedMemorySize, L_END);
  fused_decode<<<256, 512, L_END, stream>>>((const u16*)d_in[0], (const u16*)d_in[1], (const u16*)d_in[2], (const u16*)d_in[3],
                                            (const u16*)d_in[4], (const u16*)d_in[6], (const u16*)d_in[7], (const u16*)d_in[8],
                                            (u16*)d_out, (char*)d_ws + WS_OFF(ws_size));
}
```

```cpp
#define DIAG_WV 6
#define DIAG_IDX 0
#define DIAG_K 10
#define DIAG_CLK 0
#include <hip/hip_runtime.h>
#include <hip/hip_bf16.h>
#include <stdint.h>
#include <type_traits>

typedef unsigned short u16;
typedef unsigned long long u64;
typedef unsigned u32x4 __attribute__((ext_vector_type(4)));
typedef unsigned u32x2 __attribute__((ext_vector_type(2)));
typedef float f32x4 __attribute__((ext_vector_type(4)));
typedef float f32x2 __attribute__((ext_vector_type(2)));
typedef short s16x4 __attribute__((ext_vector_type(4)));
typedef short s16x8 __attribute__((ext_vector_type(8)));
typedef __bf16 bf16x8 __attribute__((ext_vector_type(8)));
typedef __bf16 bf16x2 __attribute__((ext_vector_type(2)));
#define LAS __attribute__((address_space(3)))
#define GAS __attribute__((address_space(1)))

#define THR 10
#define STAMPS 0
#define CODE_KB 0
#define LSLP 0
#define DBG 0
#define WS_OFF(s) 0
#define SLP 2

constexpr int SK = 65536, WIN = 4096, DF = 1792;
constexpr int L_WQ = 0, L_K = 16384, L_GU = 49152, L_V = 65536, L_WO = 98304, L_DN = 114688, L_MISC = 122880;
constexpr int L_XN = L_MISC, L_QA = L_XN + 1024, L_PB = L_QA + 1024, L_WS = L_PB + 1280, L_OA = L_WS + 128, L_HA = L_OA + 64,
              L_FL = L_HA + 64, L_END = L_FL + 128;
constexpr int F_CNT = L_FL, F_PF = L_FL + 16, F_POF = L_FL + 32, F_O3F = L_FL + 48, F_OAF = L_FL + 64, F_HAF = L_FL + 68;
constexpr int W_E = 0, W_QG = 4096, W_HG = W_QG + 65536, W_ML = W_HG + 65536, W_PO = W_ML + 16384 + 4096 * 3,
              W_O3 = W_PO + (1 << 20), W_END = W_O3 + (1 << 20);
constexpr int W_ST = 8 << 20;
constexpr int C_WQ = 8, C_K = 24, C_GU = 32, C_V0 = 36, C_WO = 56, C_DN = 60, C_ALL = 60;
constexpr unsigned SPIN_LDS = 1u << 22, SPIN_GLB = 1u << 17;
constexpr float LOG2E = 1.4426950408889634f;
constexpr int OOB = 0x7ffffff0;

__device__ __forceinline__ float bf_lo(unsigned w) { return __uint_as_float(w << 16); }
__device__ __forceinline__ float bf_hi(unsigned w) { return __uint_as_float(w & 0xffff0000u); }
__device__ __forceinline__ unsigned pack_bf(float lo, float hi) {
  f32x2 v = {lo, hi};
  return __builtin_bit_cast(unsigned, __builtin_convertvector(v, bf16x2));
}
__device__ __forceinline__ float rbf(float f) { return bf_lo(pack_bf(f, f)); }
__device__ __forceinline__ float rcp(float f) { return __builtin_amdgcn_rcpf(f); }
__device__ __forceinline__ float vmax(float a, float b) { float r; asm("v_max_f32 %0, %1, %2" : "=v"(r) : "v"(a), "v"(b)); return r; }
template <int CTRL> __device__ __forceinline__ float dpp(float x) {
  return __builtin_bit_cast(float, __builtin_amdgcn_mov_dpp(__builtin_bit_cast(int, x), CTRL, 0xf, 0xf, true));
}
constexpr int XOR1 = 0xB1, XOR2 = 0x4E, ROR4 = 0x124, ROR8 = 0x128;
__device__ __forceinline__ float xrow_sum(float x) {
  auto s = __builtin_amdgcn_permlane16_swap(__float_as_uint(x), __float_as_uint(x), false, false);
  x = __uint_as_float(s[0]) + __uint_as_float(s[1]);
  auto t = __builtin_amdgcn_permlane32_swap(__float_as_uint(x), __float_as_uint(x), false, false);
  return __uint_as_float(t[0]) + __uint_as_float(t[1]);
}
__device__ __forceinline__ float xrow_max(float x) {
  auto s = __builtin_amdgcn_permlane16_swap(__float_as_uint(x), __float_as_uint(x), false, false);
  x = fmaxf(__uint_as_float(s[0]), __uint_as_float(s[1]));
  auto t = __builtin_amdgcn_permlane32_swap(__float_as_uint(x), __float_as_uint(x), false, false);
  return fmaxf(__uint_as_float(t[0]), __uint_as_float(t[1]));
}
__device__ __forceinline__ float wave_sum(float x) {
  x += dpp<XOR1>(x); x += dpp<XOR2>(x); x += dpp<ROR4>(x); x += dpp<ROR8>(x);
  return xrow_sum(x);
}
__device__ __forceinline__ float wave_max(float x) {
  x = fmaxf(x, dpp<XOR1>(x)); x = fmaxf(x, dpp<XOR2>(x)); x = fmaxf(x, dpp<ROR4>(x)); x = fmaxf(x, dpp<ROR8>(x));
  return xrow_max(x);
}
__device__ __forceinline__ float sel4(int g, float a0, float a1, float a2, float a3) {
  const float lo = (g & 1) ? a1 : a0, hi = (g & 1) ? a3 : a2;
  return (g & 2) ? hi : lo;
}
__device__ __forceinline__ bool none(bool bad) { return __builtin_amdgcn_ballot_w64(bad) == 0ull; }
#define SCHED() __builtin_amdgcn_sched_barrier(0)
#define MEMBAR() asm volatile("" ::: "memory")
#define LDS_WAIT() asm volatile("s_waitcnt lgkmcnt(0)" ::: "memory")
__device__ __forceinline__ void lds_flag(int addr, unsigned v) {
  asm volatile("ds_write_b32 %0, %1" ::"v"(addr), "v"(v) : "memory");
}
__device__ __forceinline__ void wait_cnt(LAS char* L, unsigned thr) {
#pragma nounroll
  for (unsigned n = 0; n < SPIN_LDS; ++n) {
    u32x2 c = *(volatile LAS u32x2*)(L + F_CNT);
    if (__builtin_amdgcn_readfirstlane(min(c[0], c[1])) >= thr) break;
    if (LSLP) __builtin_amdgcn_s_sleep(LSLP);
  }
  MEMBAR();
}
__device__ __forceinline__ void wait_flag4(LAS char* L, int off) {
#pragma nounroll
  for (unsigned n = 0; n < SPIN_LDS; ++n) {
    u32x4 c = *(volatile LAS u32x4*)(L + off);
    if (__builtin_amdgcn_readfirstlane(c[0] & c[1] & c[2] & c[3])) break;
    if (LSLP) __builtin_amdgcn_s_sleep(LSLP);
  }
  MEMBAR();
}
__device__ __forceinline__ void wait_flag1(LAS char* L, int off) {
#pragma nounroll
  for (unsigned n = 0; n < SPIN_LDS; ++n) {
    unsigned c = *(volatile LAS unsigned*)(L + off);
    if (__builtin_amdgcn_readfirstlane(c)) break;
    if (LSLP) __builtin_amdgcn_s_sleep(LSLP);
  }
  MEMBAR();
}
__device__ __forceinline__ bf16x8 tr_pair(LAS char* L, int a0, int a1) {
  s16x4 t0 = __builtin_amdgcn_ds_read_tr16_b64_v4i16((LAS s16x4*)(L + a0));
  s16x4 t1 = __builtin_amdgcn_ds_read_tr16_b64_v4i16((LAS s16x4*)(L + a1));
  s16x8 b = {t0[0], t0[1], t0[2], t0[3], t1[0], t1[1], t1[2], t1[3]};
  return __builtin_bit_cast(bf16x8, b);
}
#define MFMA32(a, b, c) __builtin_amdgcn_mfma_f32_16x16x32_bf16((a), (b), (c), 0, 0, 0)
#define DMA(src, dst) __builtin_amdgcn_global_load_lds((const unsigned*)(src), (LAS unsigned*)(dst), 16, 0, 2)
#define SC1 16
#define LD4(off, soff) __builtin_bit_cast(u32x4, __builtin_amdgcn_raw_buffer_load_b128(wsr, (off), (soff), SC1))

template <int N, int SLEEP>
__device__ __forceinline__ void gather(__amdgpu_buffer_rsrc_t wsr, int off, int stride, unsigned tag, float (&lo)[N], float (&hi)[N]) {
  int o[N];
#pragma unroll
  for (int k = 0; k < N; ++k) { o[k] = off; lo[k] = 0.f; hi[k] = 0.f; }
#pragma nounroll
  for (unsigned n = 0; n < SPIN_GLB; ++n) {
    MEMBAR();
    u32x4 v[N];
#pragma unroll
    for (int k = 0; k < N; ++k) v[k] = LD4(o[k], k * stride);
    bool pend = false;
#pragma unroll
    for (int k = 0; k < N; ++k) {
      const bool ok = (v[k][1] == tag) & (v[k][3] == tag) & (o[k] != OOB);
      lo[k] = ok ? __uint_as_float(v[k][0]) : lo[k];
      hi[k] = ok ? __uint_as_float(v[k][2]) : hi[k];
      o[k] = ok ? OOB : o[k];
      pend |= o[k] != OOB;
    }
    if (none(pend)) break;
    if (SLEEP) __builtin_amdgcn_s_sleep(SLEEP);
  }
}

#define STAMP(k) do { unsigned t_ = (unsigned)__builtin_amdgcn_s_memrealtime(); lds_flag(L_END + (wv * 16 + (k)) * 4, t_); } while (0)

template <int J> __device__ __forceinline__ void after_issue(int cnt_addr) {
  if constexpr (J > THR) {
    asm volatile("s_waitcnt vmcnt(%0)" ::"n"(THR) : "memory");
    constexpr int LANDED = J - THR;
    if constexpr (LANDED == C_WQ || LANDED == C_K || LANDED == C_GU || (LANDED >= C_V0 && LANDED <= 48 && (LANDED & 3) == 0) ||
                  LANDED == C_WO)
      lds_flag(cnt_addr, LANDED);
  }
}
template <int LANDED> __device__ __forceinline__ void drain_to(int cnt_addr) {
  if constexpr (LANDED > C_ALL - THR) {
    asm volatile("s_waitcnt vmcnt(%0)" ::"n"(C_ALL - LANDED) : "memory");
    lds_flag(cnt_addr, LANDED);
  }
}
template <int J0, int N, typename F> __device__ __forceinline__ void issue_run(F f) {
  if constexpr (N > 0) {
    f(std::integral_constant<int, J0>{});
    issue_run<J0 + 1, N - 1>(f);
  }
}

__global__ void __launch_bounds__(512, 2) __attribute__((amdgpu_waves_per_eu(2, 2)))
fused_decode(const u16* __restrict__ x, const u16* __restrict__ kc, const u16* __restrict__ vc, const u16* __restrict__ wq,
             const u16* __restrict__ wo, const u16* __restrict__ wg, const u16* __restrict__ wu, const u16* __restrict__ wd,
             u16* __restrict__ out, char* ws) {
  extern __shared__ __attribute__((aligned(16))) char lds_raw[];
  LAS char* L = (LAS char*)lds_raw;
  const int b = blockIdx.x, tid = threadIdx.x, lane = tid & 63;
  const int wv = __builtin_amdgcn_readfirstlane(tid >> 6);
  const int h = b & 7, i = b >> 3;
  const int g = lane >> 4, c = lane & 15;
  const int q_rh = i >> 4, q_col0 = 512 * h + 32 * (i & 15);
  const int gi = i < 28 ? i : i - 4;
  const int combo = (gi >> 1) * 8 + h;
  const int u_mat = (combo >> 1) & 1, u_rh = combo & 1, u_col0 = (combo >> 2) * 64 + (gi & 1) * 32;
  const int rg = h * 16 + (i >> 1), ch = i & 1;
  STAMP(0);

  if (wv >= 2) {
  if (wv == 7 && lane < 32) *(LAS unsigned*)(L + L_FL + lane * 4) = 0u;
  __builtin_amdgcn_s_setprio(2);
  const __amdgpu_buffer_rsrc_t wsr = __builtin_amdgcn_make_buffer_rsrc((void*)ws, 0, W_END, 0x00020000);
  const u32x4 xv = *(const GAS u32x4*)(x + lane * 8);
  const unsigned e_in = __hip_atomic_load((GAS unsigned*)(ws + W_E), __ATOMIC_RELAXED, __HIP_MEMORY_SCOPE_AGENT);
  SCHED();
  LDS_WAIT();
  __builtin_amdgcn_s_barrier();
  SCHED();

  if (wv < 6) {
    const int cw = wv - 2;
    {
      float f[8];
#pragma unroll
      for (int k = 0; k < 4; ++k) { f[2 * k] = bf_lo(xv[k]); f[2 * k + 1] = bf_hi(xv[k]); }
      float s = ((f[0] + f[1]) + (f[2] + f[3])) + ((f[4] + f[5]) + (f[6] + f[7]));
      const float mean = wave_sum(s) * (1.f / 512.f);
      float v0 = 0.f, v1 = 0.f;
#pragma unroll
      for (int k = 0; k < 8; k += 2) { const float d0 = f[k] - mean, d1 = f[k + 1] - mean; v0 = fmaf(d0, d0, v0); v1 = fmaf(d1, d1, v1); }
      const float var = wave_sum(v0 + v1) * (1.f / 512.f);
      const float mb = rbf(mean);
      const float den = rbf(__builtin_amdgcn_sqrtf(rbf(rbf(var) + 1e-5f)));
      const float inv = rcp(den);
      u32x4 xn;
#pragma unroll
      for (int k = 0; k < 4; ++k) {
        const unsigned d = pack_bf(f[2 * k] - mb, f[2 * k + 1] - mb);
        float a0 = bf_lo(d), a1 = bf_hi(d);
        float q0 = a0 * inv, q1 = a1 * inv;
        q0 = fmaf(fmaf(-q0, den, a0), inv, q0); q1 = fmaf(fmaf(-q1, den, a1), inv, q1);
        xn[k] = pack_bf(q0, q1);
      }
      *(LAS u32x4*)(L + L_XN + lane * 16) = xn;
    }
    const unsigned tag = __builtin_amdgcn_readfirstlane(e_in) * 1664525u + 1013904223u;
    STAMP(1);
    const int q4 = (lane & 15) >> 2, pp = lane & 3;
    const int p_tile = cw < 2 ? L_WQ : L_GU, p_rh = cw < 2 ? q_rh : u_rh;
    bf16x8 xa[8];
#pragma unroll
    for (int ks = 0; ks < 8; ++ks) xa[ks] = *(LAS bf16x8*)(L + L_XN + p_rh * 512 + g * 16 + ks * 64);
    auto proj = [&]() -> float {
      const int tb = p_tile + (8 * g + q4) * 64 + (((2 * (cw & 1) + (pp >> 1)) ^ ((g & 1) << 1)) * 16) + 8 * (pp & 1);
      bf16x8 wb[8];
#pragma unroll
      for (int ks = 0; ks < 8; ++ks) wb[ks] = tr_pair(L, tb + ks * 2048, tb + ks * 2048 + 256);
      SCHED();
      f32x4 a0 = {0.f, 0.f, 0.f, 0.f}, a1 = {0.f, 0.f, 0.f, 0.f};
#pragma unroll
      for (int ks = 0; ks < 8; ks += 2) { a0 = MFMA32(xa[ks], wb[ks], a0); a1 = MFMA32(xa[ks + 1], wb[ks + 1], a1); }
      return a0[0] + a1[0];
    };
    if (cw < 2) {
      wait_cnt(L, C_WQ);
      STAMP(2);
      const float qp = proj();
      if (lane < 16)
        __hip_atomic_store((GAS u64*)(ws + W_QG + (size_t)(q_rh * 4096 + q_col0 + 16 * cw + lane) * 8),
                           ((u64)tag << 32) | __float_as_uint(qp), __ATOMIC_RELAXED, __HIP_MEMORY_SCOPE_AGENT);
      STAMP(3);
    }
    wait_cnt(L, C_K);
    STAMP(4);
    {
      bf16x8 kb[8][4];
#pragma unroll
      for (int kt = 0; kt < 8; ++kt)
#pragma unroll
        for (int ks = 0; ks < 4; ++ks) kb[kt][ks] = *(LAS bf16x8*)(L + L_K + kt * 4096 + c * 256 + (((4 * ks + g) ^ c) * 16));
      SCHED();
      float qlo[2], qhi[2];
      gather<2, SLP>(wsr, W_QG + ((4 * h + cw) * 128 + 2 * lane) * 8, 32768, tag, qlo, qhi);
      STAMP(5);
      *(LAS unsigned*)(L + L_QA + cw * 256 + lane * 4) = pack_bf(qlo[0] + qlo[1], qhi[0] + qhi[1]);
      bf16x8 qa[4];
#pragma unroll
      for (int ks = 0; ks < 4; ++ks) qa[ks] = *(LAS bf16x8*)(L + L_QA + cw * 256 + ks * 64 + g * 16);
      SCHED();
      f32x4 sc[8];
#pragma unroll
      for (int kt = 0; kt < 8; ++kt) sc[kt] = MFMA32(qa[0], kb[kt][0], (f32x4{0.f, 0.f, 0.f, 0.f}));
#pragma unroll
      for (int ks = 1; ks < 4; ++ks)
#pragma unroll
        for (int kt = 0; kt < 8; ++kt) sc[kt] = MFMA32(qa[ks], kb[kt][ks], sc[kt]);
      float s0 = sel4(g, sc[0][0], sc[2][0], sc[4][0], sc[6][0]);
      float s1 = sel4(g, sc[1][0], sc[3][0], sc[5][0], sc[7][0]);
      unsigned r = pack_bf(s0, s1);
      r = pack_bf(bf_lo(r) * 0.08838834764831845f, bf_hi(r) * 0.08838834764831845f);
      s0 = bf_lo(r); s1 = bf_hi(r);
      const float m = wave_max(fmaxf(s0, s1));
      const float p0 = __builtin_amdgcn_exp2f((s0 - m) * LOG2E), p1 = __builtin_amdgcn_exp2f((s1 - m) * LOG2E);
      const unsigned pb = pack_bf(p0, p1);
      *(LAS u16*)(L + L_PB + cw * 320 + (32 * g + c) * 2) = (u16)pb;
      *(LAS u16*)(L + L_PB + cw * 320 + (32 * g + 16 + c) * 2) = (u16)(pb >> 16);
      const float l = wave_sum(p0 + p1);
      LDS_WAIT();
      lds_flag(F_PF + 4 * cw, 1);
      if (lane == 0) {
        const u32x4 ml = {__float_as_uint(m), tag, __float_as_uint(l), tag};
        __builtin_amdgcn_raw_buffer_store_b128(ml, wsr, W_ML + ((4 * h + cw) * 32 + i) * 16, 0, SC1);
      }
    }
    STAMP(6);
    if (cw >= 2) {
      wait_cnt(L, C_GU);
      const float up = proj();
      if (lane < 16 && i < 28)
        __hip_atomic_store((GAS u64*)(ws + W_HG + (size_t)((u_mat * 2 + u_rh) * DF + u_col0 + 16 * (cw - 2) + lane) * 8),
                           ((u64)tag << 32) | __float_as_uint(up), __ATOMIC_RELAXED, __HIP_MEMORY_SCOPE_AGENT);
      STAMP(7);
    }
    {
      wait_flag4(L, F_PF);
      bf16x8 pa[4];
#pragma unroll
      for (int ks = 0; ks < 4; ++ks) pa[ks] = *(LAS bf16x8*)(L + L_PB + (c >> 2) * 320 + ks * 64 + g * 16);
      const int f = q4 | ((g & 1) << 2);
      const int vb0 = L_V + (8 * g + q4) * 256 + (((2 * cw) ^ f) * 32) + pp * 8;
      const int vb1 = L_V + (8 * g + q4) * 256 + (((2 * cw + 1) ^ f) * 32) + pp * 8;
      f32x4 o0 = {0.f, 0.f, 0.f, 0.f}, o1 = {0.f, 0.f, 0.f, 0.f};
#pragma unroll
      for (int ks = 0; ks < 4; ++ks) {
        wait_cnt(L, C_V0 + 4 * ks);
        const bf16x8 b0 = tr_pair(L, vb0 + ks * 8192, vb0 + ks * 8192 + 1024);
        const bf16x8 b1 = tr_pair(L, vb1 + ks * 8192, vb1 + ks * 8192 + 1024);
        SCHED();
        o0 = MFMA32(pa[ks], b0, o0);
        o1 = MFMA32(pa[ks], b1, o1);
      }
      const u32x4 po = {__float_as_uint(o0[0]), tag, __float_as_uint(o1[0]), tag};
      __builtin_amdgcn_raw_buffer_store_b128(po, wsr, W_PO + (((4 * h + g) * 32 + i) * 128 + 32 * cw + 2 * c) * 8, 0, SC1);
      lds_flag(F_POF + 4 * cw, 1);
    }
    STAMP(9);
    {
      const int f = q4 | ((g & 1) << 2), fd = (4 * g + q4) & 7;
      bf16x8 wb[4];
      s16x4 db[4];
      wait_cnt(L, C_WO);
#pragma unroll
      for (int u = 0; u < 4; ++u) {
        const int a = L_WO + (8 * g + q4) * 512 + (((4 * cw + u) ^ f) * 32) + pp * 8;
        wb[u] = tr_pair(L, a, a + 2048);
      }
      wait_cnt(L, C_DN);
      STAMP(10);
#pragma unroll
      for (int u = 0; u < 4; ++u)
        db[u] = __builtin_amdgcn_ds_read_tr16_b64_v4i16((LAS s16x4*)(L + L_DN + (4 * g + q4) * 512 + (((4 * cw + u) ^ fd) * 32) + pp * 8));
      wait_flag1(L, F_HAF);
      const s16x4 ha = *(LAS s16x4*)(L + L_HA + g * 8);
      f32x4 dd[4], ss[4];
#pragma unroll
      for (int u = 0; u < 4; ++u) dd[u] = __builtin_amdgcn_mfma_f32_16x16x16bf16_1k(ha, db[u], f32x4{0.f, 0.f, 0.f, 0.f}, 0, 0, 0);
      const float dsel = sel4(g, dd[0][0], dd[1][0], dd[2][0], dd[3][0]);
      SCHED();
      wait_flag1(L, F_OAF);
      const bf16x8 oa = *(LAS bf16x8*)(L + L_OA + g * 16);
#pragma unroll
      for (int u = 0; u < 4; ++u) ss[u] = MFMA32(oa, wb[u], (f32x4{0.f, 0.f, 0.f, 0.f}));
      const float ssel = sel4(g, ss[0][0], ss[1][0], ss[2][0], ss[3][0]);
      const u32x4 o3 = {__float_as_uint(ssel), tag, __float_as_uint(dsel), tag};
      __builtin_amdgcn_raw_buffer_store_b128(o3, wsr, W_O3 + (rg * 512 + ch * 256 + cw * 64 + lane) * 16, 0, SC1);
      lds_flag(F_O3F + 4 * cw, 1);
    }
    STAMP(12);
    return;
  }

  if (wv == 6) {
    const int col0 = (h * 8 + (i >> 2)) * 8 + 2 * (i & 3);
    const unsigned xw = __builtin_amdgcn_readlane(xv[i & 3], col0 >> 3);
    const unsigned tag = __builtin_amdgcn_readfirstlane(e_in) * 1664525u + 1013904223u;
    const int r = rg >> 2, dc = rg & 3;
    wait_flag4(L, F_PF);
    wait_cnt(L, C_V0 + 4);
    STAMP(1);
    float w[8];
    {
      float m[1], l[1];
      gather<1, SLP>(wsr, W_ML + (r * 32 + (lane & 31)) * 16, 0, tag, m, l);
      const float M = wave_max(m[0]);
      const float e = __builtin_amdgcn_exp2f((m[0] - M) * LOG2E);
      const float Lsum = wave_sum(e * l[0]) * 0.5f;
      *(LAS float*)(L + L_WS + ((lane & 3) * 8 + ((lane & 31) >> 2)) * 4) = e * rcp(Lsum);
      const f32x4 w0 = *(LAS f32x4*)(L + L_WS + g * 32), w1 = *(LAS f32x4*)(L + L_WS + g * 32 + 16);
#pragma unroll
      for (int k = 0; k < 4; ++k) { w[k] = w0[k]; w[4 + k] = w1[k]; }
    }
    STAMP(2);
    wait_flag4(L, F_POF);
    STAMP(3);
    {
      float v0[8], v1[8];
      gather<8, 0>(wsr, W_PO + ((r * 32 + g) * 128 + 32 * dc + 2 * c) * 8, 4096, tag, v0, v1);
      STAMP(4);
      float a0 = 0.f, a1 = 0.f;
#pragma unroll
      for (int k = 0; k < 8; ++k) { a0 = fmaf(w[k], v0[k], a0); a1 = fmaf(w[k], v1[k], a1); }
      a0 = xrow_sum(a0); a1 = xrow_sum(a1);
      const unsigned ob = pack_bf(a0, a1);
      *(LAS u16*)(L + L_OA + c * 2) = (u16)ob;
      *(LAS u16*)(L + L_OA + 32 + c * 2) = (u16)(ob >> 16);
      LDS_WAIT();
      lds_flag(F_OAF, 1);
    }
    wait_flag4(L, F_O3F);
    STAMP(6);
    {
      float vs[4], vd[4];
      gather<4, 0>(wsr, W_O3 + ((lane >> 1) * 512 + col0 + (lane & 1)) * 16, 32 * 8192, tag, vs, vd);
      STAMP(7);
      float s = (vs[0] + vs[1]) + (vs[2] + vs[3]);
      float d = (vd[0] + vd[1]) + (vd[2] + vd[3]);
      s += dpp<XOR2>(s); d += dpp<XOR2>(d);
      s += dpp<ROR4>(s); d += dpp<ROR4>(d);
      s += dpp<ROR8>(s); d += dpp<ROR8>(d);
      s = xrow_sum(s); d = xrow_sum(d);
      const unsigned sd = pack_bf(s, d);
      s = bf_lo(sd); d = bf_hi(sd);
      const float gt = rbf(d * rcp(1.f + __builtin_amdgcn_exp2f(-d * LOG2E)));
      const float xe = (lane & 1) ? bf_hi(xw) : bf_lo(xw);
      const unsigned res = pack_bf(xe + rbf(s * gt), 0.f);
      if (lane < 2) *(GAS u16*)(out + col0 + lane) = (u16)res;
      if (b == 0 && lane == 0) __hip_atomic_store((GAS unsigned*)(ws + W_E), tag, __ATOMIC_RELAXED, __HIP_MEMORY_SCOPE_AGENT);
    }
    STAMP(8);
    {
      for (int z = 0; z < 8; ++z) __builtin_amdgcn_s_sleep(64);
      LDS_WAIT();
      unsigned t0 = *(volatile LAS unsigned*)(L + L_END + (DIAG_WV * 16 + 0) * 4);
      unsigned t1 = *(volatile LAS unsigned*)(L + L_END + (DIAG_WV * 16 + DIAG_IDX) * 4);
      unsigned val = __builtin_amdgcn_readfirstlane(t1 - t0);
      if (val > 5000u) val = 5000u;
      const unsigned s0 = (unsigned)__builtin_amdgcn_s_memrealtime();
      while ((unsigned)__builtin_amdgcn_s_memrealtime() - s0 < val * DIAG_K) __builtin_amdgcn_s_sleep(8);
    }
    return;
  }

  {
    const unsigned tag = __builtin_amdgcn_readfirstlane(e_in) * 1664525u + 1013904223u;
    wait_cnt(L, C_V0 + 8);
    STAMP(1);
    const GAS u64* src = (const GAS u64*)(ws + W_HG + (size_t)(g * DF + rg * 14 + min(c, 13)) * 8);
    u64 v;
#pragma nounroll
    for (unsigned n = 0; n < SPIN_GLB; ++n) {
      v = __hip_atomic_load(src, __ATOMIC_RELAXED, __HIP_MEMORY_SCOPE_AGENT);
      if (none((unsigned)(v >> 32) != tag)) break;
      __builtin_amdgcn_s_sleep(SLP);
    }
    STAMP(2);
    auto s = __builtin_amdgcn_permlane16_swap((unsigned)v, (unsigned)v, false, false);
    const float y = __uint_as_float(s[0]) + __uint_as_float(s[1]);
    auto t = __builtin_amdgcn_permlane32_swap(__float_as_uint(y), __float_as_uint(y), false, false);
    const unsigned gu = pack_bf(__uint_as_float(t[0]), __uint_as_float(t[1]));
    const float gg = bf_lo(gu), uu = bf_hi(gu);
    const float z = 0.7978845608028654f * (gg + 0.044715f * gg * gg * gg);
    const float ge = rbf(gg * rcp(1.f + __builtin_amdgcn_exp2f(-2.f * LOG2E * z)));
    const unsigned hb = c < 14 ? pack_bf(ge * uu, 0.f) : 0u;
    *(LAS u16*)(L + L_HA + c * 2) = (u16)hb;
    LDS_WAIT();
    lds_flag(F_HAF, 1);
    STAMP(3);
  }
  return;
  }

  {
    const int lw = wv;
    const int cnt_addr = F_CNT + 4 * lw;
    const size_t hoff = ((size_t)h * SK + (SK - WIN)) * 256;
    {
      const int row = lane >> 2, chunk = (lane & 3) ^ (((row >> 3) & 1) << 1);
      const char* src = (const char*)wq + (size_t)(q_rh * 256 + lw * 16) * 8192 + q_col0 * 2;
      const unsigned vo = row * 8192 + chunk * 16;
      issue_run<0, 8>([&](auto J) {
        constexpr int j = decltype(J)::value;
        DMA(src + (size_t)j * 32 * 8192 + vo, L + L_WQ + (2 * j + lw) * 1024);
        after_issue<j + 1>(cnt_addr);
      });
    }
    __builtin_amdgcn_s_barrier();
    {
      const char* src = (const char*)kc + hoff + lw * 1024;
      const unsigned vo0 = g * 256 + ((c ^ (4 * lw + g)) * 16), vo1 = g * 256 + ((c ^ (4 * (lw + 2) + g)) * 16) + 2048;
      issue_run<0, 16>([&](auto J) {
        constexpr int j = decltype(J)::value, blk = j >> 1;
        const char* s = src + (size_t)(blk * 32 + (i ^ ((blk & 1) << 1))) * 4096;
        DMA(s + ((j & 1) ? vo1 : vo0), L + L_K + (2 * j + lw) * 1024);
        after_issue<8 + j + 1>(cnt_addr);
      });
    }
    {
      const int row = lane >> 2, chunk = (lane & 3) ^ (((row >> 3) & 1) << 1);
      const char* src = (const char*)(u_mat ? wu : wg) + (size_t)(u_rh * 256 + lw * 16) * (DF * 2) + u_col0 * 2;
      const unsigned vo = row * (DF * 2) + chunk * 16;
      issue_run<0, 8>([&](auto J) {
        constexpr int j = decltype(J)::value;
        DMA(src + (size_t)j * 32 * (DF * 2) + vo, L + L_GU + (2 * j + lw) * 1024);
        after_issue<24 + j + 1>(cnt_addr);
      });
    }
    {
      const char* src = (const char*)vc + hoff + lw * 1024;
      const unsigned vo0 = g * 256 + ((c ^ (g << 1)) * 16), vo1 = g * 256 + ((c ^ ((g | 4) << 1)) * 16) + 2048;
      issue_run<0, 16>([&](auto J) {
        constexpr int j = decltype(J)::value, blk = j >> 1;
        const char* s = src + (size_t)(blk * 32 + (i ^ ((blk & 1) << 1))) * 4096;
        DMA(s + ((j & 1) ? vo1 : vo0), L + L_V + (2 * j + lw) * 1024);
        after_issue<32 + j + 1>(cnt_addr);
      });
    }
    {
      const int r2 = lane >> 5, pos = lane & 31;
      const char* src = (const char*)wo + (size_t)(rg * 32 + 2 * lw) * 1024 + ch * 512;
      const unsigned vo0 = r2 * 1024 + ((pos ^ ((2 * lw + r2) << 1)) * 16), vo1 = r2 * 1024 + ((pos ^ (((2 * lw + r2) | 4) << 1)) * 16);
      issue_run<0, 8>([&](auto J) {
        constexpr int j = decltype(J)::value;
        DMA(src + j * 4096 + (((j >> 1) & 1) ? vo1 : vo0), L + L_WO + (2 * j + lw) * 1024);
        after_issue<48 + j + 1>(cnt_addr);
      });
      const char* srd = (const char*)wd + (size_t)(rg * 14 + 2 * lw) * 1024 + ch * 512;
      issue_run<0, 4>([&](auto J) {
        constexpr int j = decltype(J)::value;
        const int back = (j == 3) ? lw * 2048 : 0;
        DMA(srd + j * 4096 - back + ((j & 1) ? vo1 : vo0), L + L_DN + (2 * j + lw) * 1024);
        after_issue<56 + j + 1>(cnt_addr);
      });
    }
    drain_to<C_WQ>(cnt_addr); drain_to<C_K>(cnt_addr); drain_to<C_GU>(cnt_addr);
    drain_to<36>(cnt_addr); drain_to<40>(cnt_addr); drain_to<44>(cnt_addr); drain_to<48>(cnt_addr);
    drain_to<C_WO>(cnt_addr); drain_to<C_DN>(cnt_addr);
    STAMP(2);
  }
}

extern "C" void kernel_launch(void* const* d_in, const int* in_sizes, int n_in, void* d_out, int out_size, void* d_ws,
                              size_t ws_size, hipStream_t stream) {
  (void)in_sizes; (void)n_in; (void)out_size; (void)ws_size;
  (void)hipFuncSetAttribute((const void*)fused_decode, hipFuncAttributeMaxDynamicSharedMemorySize, L_END + 512);
  fused_decode<<<256, 512, L_END + 512, stream>>>((const u16*)d_in[0], (const u16*)d_in[1], (const u16*)d_in[2], (const u16*)d_in[3],
                                            (const u16*)d_in[4], (const u16*)d_in[6], (const u16*)d_in[7], (const u16*)d_in[8],
                                            (u16*)d_out, (char*)d_ws + WS_OFF(ws_size));
}
```

```cpp
#define DIAG_WV 6
#define DIAG_IDX 0
#define DIAG_IDX0 0
#define DIAG_K 10
#define DIAG_CLK 0
#define WBL2 2
#include <hip/hip_runtime.h>
#include <hip/hip_bf16.h>
#include <stdint.h>
#include <type_traits>

typedef unsigned short u16;
typedef unsigned long long u64;
typedef unsigned u32x4 __attribute__((ext_vector_type(4)));
typedef unsigned u32x2 __attribute__((ext_vector_type(2)));
typedef float f32x4 __attribute__((ext_vector_type(4)));
typedef float f32x2 __attribute__((ext_vector_type(2)));
typedef short s16x4 __attribute__((ext_vector_type(4)));
typedef short s16x8 __attribute__((ext_vector_type(8)));
typedef __bf16 bf16x8 __attribute__((ext_vector_type(8)));
typedef __bf16 bf16x2 __attribute__((ext_vector_type(2)));
#define LAS __attribute__((address_space(3)))
#define GAS __attribute__((address_space(1)))

#define THR 10
#define STAMPS 0
#define CODE_KB 0
#define LSLP 0
#define DBG 0
#define WS_OFF(s) 0
#define SLP 2

constexpr int SK = 65536, WIN = 4096, DF = 1792;
constexpr int L_WQ = 0, L_K = 16384, L_GU = 49152, L_V = 65536, L_WO = 98304, L_DN = 114688, L_MISC = 122880;
constexpr int L_XN = L_MISC, L_QA = L_XN + 1024, L_PB = L_QA + 1024, L_WS = L_PB + 1280, L_OA = L_WS + 128, L_HA = L_OA + 64,
              L_FL = L_HA + 64, L_END = L_FL + 128;
constexpr int F_CNT = L_FL, F_PF = L_FL + 16, F_POF = L_FL + 32, F_O3F = L_FL + 48, F_OAF = L_FL + 64, F_HAF = L_FL + 68;
constexpr int W_E = 0, W_QG = 4096, W_HG = W_QG + 65536, W_ML = W_HG + 65536, W_PO = W_ML + 16384 + 4096 * 3,
              W_O3 = W_PO + (1 << 20), W_END = W_O3 + (1 << 20);
constexpr int W_ST = 8 << 20;
constexpr int C_WQ = 8, C_K = 24, C_GU = 32, C_V0 = 36, C_WO = 56, C_DN = 60, C_ALL = 60;
constexpr unsigned SPIN_LDS = 1u << 22, SPIN_GLB = 1u << 17;
constexpr float LOG2E = 1.4426950408889634f;
constexpr int OOB = 0x7ffffff0;

__device__ __forceinline__ float bf_lo(unsigned w) { return __uint_as_float(w << 16); }
__device__ __forceinline__ float bf_hi(unsigned w) { return __uint_as_float(w & 0xffff0000u); }
__device__ __forceinline__ unsigned pack_bf(float lo, float hi) {
  f32x2 v = {lo, hi};
  return __builtin_bit_cast(unsigned, __builtin_convertvector(v, bf16x2));
}
__device__ __forceinline__ float rbf(float f) { return bf_lo(pack_bf(f, f)); }
__device__ __forceinline__ float rcp(float f) { return __builtin_amdgcn_rcpf(f); }
__device__ __forceinline__ float vmax(float a, float b) { float r; asm("v_max_f32 %0, %1, %2" : "=v"(r) : "v"(a), "v"(b)); return r; }
template <int CTRL> __device__ __forceinline__ float dpp(float x) {
  return __builtin_bit_cast(float, __builtin_amdgcn_mov_dpp(__builtin_bit_cast(int, x), CTRL, 0xf, 0xf, true));
}
constexpr int XOR1 = 0xB1, XOR2 = 0x4E, ROR4 = 0x124, ROR8 = 0x128;
__device__ __forceinline__ float xrow_sum(float x) {
  auto s = __builtin_amdgcn_permlane16_swap(__float_as_uint(x), __float_as_uint(x), false, false);
  x = __uint_as_float(s[0]) + __uint_as_float(s[1]);
  auto t = __builtin_amdgcn_permlane32_swap(__float_as_uint(x), __float_as_uint(x), false, false);
  return __uint_as_float(t[0]) + __uint_as_float(t[1]);
}
__device__ __forceinline__ float xrow_max(float x) {
  auto s = __builtin_amdgcn_permlane16_swap(__float_as_uint(x), __float_as_uint(x), false, false);
  x = fmaxf(__uint_as_float(s[0]), __uint_as_float(s[1]));
  auto t = __builtin_amdgcn_permlane32_swap(__float_as_uint(x), __float_as_uint(x), false, false);
  return fmaxf(__uint_as_float(t[0]), __uint_as_float(t[1]));
}
__device__ __forceinline__ float wave_sum(float x) {
  x += dpp<XOR1>(x); x += dpp<XOR2>(x); x += dpp<ROR4>(x); x += dpp<ROR8>(x);
  return xrow_sum(x);
}
__device__ __forceinline__ float wave_max(float x) {
  x = fmaxf(x, dpp<XOR1>(x)); x = fmaxf(x, dpp<XOR2>(x)); x = fmaxf(x, dpp<ROR4>(x)); x = fmaxf(x, dpp<ROR8>(x));
  return xrow_max(x);
}
__device__ __forceinline__ float sel4(int g, float a0, float a1, float a2, float a3) {
  const float lo = (g & 1) ? a1 : a0, hi = (g & 1) ? a3 : a2;
  return (g & 2) ? hi : lo;
}
__device__ __forceinline__ bool none(bool bad) { return __builtin_amdgcn_ballot_w64(bad) == 0ull; }
#define SCHED() __builtin_amdgcn_sched_barrier(0)
#define MEMBAR() asm volatile("" ::: "memory")
#define LDS_WAIT() asm volatile("s_waitcnt lgkmcnt(0)" ::: "memory")
__device__ __forceinline__ void lds_flag(int addr, unsigned v) {
  asm volatile("ds_write_b32 %0, %1" ::"v"(addr), "v"(v) : "memory");
}
__device__ __forceinline__ void wait_cnt(LAS char* L, unsigned thr) {
#pragma nounroll
  for (unsigned n = 0; n < SPIN_LDS; ++n) {
    u32x2 c = *(volatile LAS u32x2*)(L + F_CNT);
    if (__builtin_amdgcn_readfirstlane(min(c[0], c[1])) >= thr) break;
    if (LSLP) __builtin_amdgcn_s_sleep(LSLP);
  }
  MEMBAR();
}
__device__ __forceinline__ void wait_flag4(LAS char* L, int off) {
#pragma nounroll
  for (unsigned n = 0; n < SPIN_LDS; ++n) {
    u32x4 c = *(volatile LAS u32x4*)(L + off);
    if (__builtin_amdgcn_readfirstlane(c[0] & c[1] & c[2] & c[3])) break;
    if (LSLP) __builtin_amdgcn_s_sleep(LSLP);
  }
  MEMBAR();
}
__device__ __forceinline__ void wait_flag1(LAS char* L, int off) {
#pragma nounroll
  for (unsigned n = 0; n < SPIN_LDS; ++n) {
    unsigned c = *(volatile LAS unsigned*)(L + off);
    if (__builtin_amdgcn_readfirstlane(c)) break;
    if (LSLP) __builtin_amdgcn_s_sleep(LSLP);
  }
  MEMBAR();
}
__device__ __forceinline__ bf16x8 tr_pair(LAS char* L, int a0, int a1) {
  s16x4 t0 = __builtin_amdgcn_ds_read_tr16_b64_v4i16((LAS s16x4*)(L + a0));
  s16x4 t1 = __builtin_amdgcn_ds_read_tr16_b64_v4i16((LAS s16x4*)(L + a1));
  s16x8 b = {t0[0], t0[1], t0[2], t0[3], t1[0], t1[1], t1[2], t1[3]};
  return __builtin_bit_cast(bf16x8, b);
}
#define MFMA32(a, b, c) __builtin_amdgcn_mfma_f32_16x16x32_bf16((a), (b), (c), 0, 0, 0)
#define DMA(src, dst) __builtin_amdgcn_global_load_lds((const unsigned*)(src), (LAS unsigned*)(dst), 16, 0, 2)
#define SC1 16
#define LD4(off, soff) __builtin_bit_cast(u32x4, __builtin_amdgcn_raw_buffer_load_b128(wsr, (off), (soff), SC1))

template <int N, int SLEEP>
__device__ __forceinline__ void gather(__amdgpu_buffer_rsrc_t wsr, int off, int stride, unsigned tag, float (&lo)[N], float (&hi)[N]) {
  int o[N];
#pragma unroll
  for (int k = 0; k < N; ++k) { o[k] = off; lo[k] = 0.f; hi[k] = 0.f; }
#pragma nounroll
  for (unsigned n = 0; n < SPIN_GLB; ++n) {
    MEMBAR();
    u32x4 v[N];
#pragma unroll
    for (int k = 0; k < N; ++k) v[k] = LD4(o[k], k * stride);
    bool pend = false;
#pragma unroll
    for (int k = 0; k < N; ++k) {
      const bool ok = (v[k][1] == tag) & (v[k][3] == tag) & (o[k] != OOB);
      lo[k] = ok ? __uint_as_float(v[k][0]) : lo[k];
      hi[k] = ok ? __uint_as_float(v[k][2]) : hi[k];
      o[k] = ok ? OOB : o[k];
      pend |= o[k] != OOB;
    }
    if (none(pend)) break;
    if (SLEEP) __builtin_amdgcn_s_sleep(SLEEP);
  }
}

#define STAMP(k) do { unsigned t_ = (unsigned)__builtin_amdgcn_s_memrealtime(); lds_flag(L_END + (wv * 16 + (k)) * 4, t_); } while (0)

template <int J> __device__ __forceinline__ void after_issue(int cnt_addr) {
  if constexpr (J > THR) {
    asm volatile("s_waitcnt vmcnt(%0)" ::"n"(THR) : "memory");
    constexpr int LANDED = J - THR;
    if constexpr (LANDED == C_WQ || LANDED == C_K || LANDED == C_GU || (LANDED >= C_V0 && LANDED <= 48 && (LANDED & 3) == 0) ||
                  LANDED == C_WO)
      lds_flag(cnt_addr, LANDED);
  }
}
template <int LANDED> __device__ __forceinline__ void drain_to(int cnt_addr) {
  if constexpr (LANDED > C_ALL - THR) {
    asm volatile("s_waitcnt vmcnt(%0)" ::"n"(C_ALL - LANDED) : "memory");
    lds_flag(cnt_addr, LANDED);
  }
}
template <int J0, int N, typename F> __device__ __forceinline__ void issue_run(F f) {
  if constexpr (N > 0) {
    f(std::integral_constant<int, J0>{});
    issue_run<J0 + 1, N - 1>(f);
  }
}

__global__ void __launch_bounds__(512, 2) __attribute__((amdgpu_waves_per_eu(2, 2)))
fused_decode(const u16* __restrict__ x, const u16* __restrict__ kc, const u16* __restrict__ vc, const u16* __restrict__ wq,
             const u16* __restrict__ wo, const u16* __restrict__ wg, const u16* __restrict__ wu, const u16* __restrict__ wd,
             u16* __restrict__ out, char* ws) {
  extern __shared__ __attribute__((aligned(16))) char lds_raw[];
  LAS char* L = (LAS char*)lds_raw;
  const int b = blockIdx.x, tid = threadIdx.x, lane = tid & 63;
  const int wv = __builtin_amdgcn_readfirstlane(tid >> 6);
  const int h = b & 7, i = b >> 3;
  const int g = lane >> 4, c = lane & 15;
  const int q_rh = i >> 4, q_col0 = 512 * h + 32 * (i & 15);
  const int gi = i < 28 ? i : i - 4;
  const int combo = (gi >> 1) * 8 + h;
  const int u_mat = (combo >> 1) & 1, u_rh = combo & 1, u_col0 = (combo >> 2) * 64 + (gi & 1) * 32;
  const int rg = h * 16 + (i >> 1), ch = i & 1;
  STAMP(0);

  if (wv >= 2) {
  if (wv == 7 && lane < 32) *(LAS unsigned*)(L + L_FL + lane * 4) = 0u;
  __builtin_amdgcn_s_setprio(2);
  const __amdgpu_buffer_rsrc_t wsr = __builtin_amdgcn_make_buffer_rsrc((void*)ws, 0, W_END, 0x00020000);
  const u32x4 xv = *(const GAS u32x4*)(x + lane * 8);
  const unsigned e_in = __hip_atomic_load((GAS unsigned*)(ws + W_E), __ATOMIC_RELAXED, __HIP_MEMORY_SCOPE_AGENT);
  SCHED();
  LDS_WAIT();
  __builtin_amdgcn_s_barrier();
  SCHED();

  if (wv < 6) {
    const int cw = wv - 2;
    {
      float f[8];
#pragma unroll
      for (int k = 0; k < 4; ++k) { f[2 * k] = bf_lo(xv[k]); f[2 * k + 1] = bf_hi(xv[k]); }
      float s = ((f[0] + f[1]) + (f[2] + f[3])) + ((f[4] + f[5]) + (f[6] + f[7]));
      const float mean = wave_sum(s) * (1.f / 512.f);
      float v0 = 0.f, v1 = 0.f;
#pragma unroll
      for (int k = 0; k < 8; k += 2) { const float d0 = f[k] - mean, d1 = f[k + 1] - mean; v0 = fmaf(d0, d0, v0); v1 = fmaf(d1, d1, v1); }
      const float var = wave_sum(v0 + v1) * (1.f / 512.f);
      const float mb = rbf(mean);
      const float den = rbf(__builtin_amdgcn_sqrtf(rbf(rbf(var) + 1e-5f)));
      const float inv = rcp(den);
      u32x4 xn;
#pragma unroll
      for (int k = 0; k < 4; ++k) {
        const unsigned d = pack_bf(f[2 * k] - mb, f[2 * k + 1] - mb);
        float a0 = bf_lo(d), a1 = bf_hi(d);
        float q0 = a0 * inv, q1 = a1 * inv;
        q0 = fmaf(fmaf(-q0, den, a0), inv, q0); q1 = fmaf(fmaf(-q1, den, a1), inv, q1);
        xn[k] = pack_bf(q0, q1);
      }
      *(LAS u32x4*)(L + L_XN + lane * 16) = xn;
    }
    const unsigned tag = __builtin_amdgcn_readfirstlane(e_in) * 1664525u + 1013904223u;
    STAMP(1);
    const int q4 = (lane & 15) >> 2, pp = lane & 3;
    const int p_tile = cw < 2 ? L_WQ : L_GU, p_rh = cw < 2 ? q_rh : u_rh;
    bf16x8 xa[8];
#pragma unroll
    for (int ks = 0; ks < 8; ++ks) xa[ks] = *(LAS bf16x8*)(L + L_XN + p_rh * 512 + g * 16 + ks * 64);
    auto proj = [&]() -> float {
      const int tb = p_tile + (8 * g + q4) * 64 + (((2 * (cw & 1) + (pp >> 1)) ^ ((g & 1) << 1)) * 16) + 8 * (pp & 1);
      bf16x8 wb[8];
#pragma unroll
      for (int ks = 0; ks < 8; ++ks) wb[ks] = tr_pair(L, tb + ks * 2048, tb + ks * 2048 + 256);
      SCHED();
      f32x4 a0 = {0.f, 0.f, 0.f, 0.f}, a1 = {0.f, 0.f, 0.f, 0.f};
#pragma unroll
      for (int ks = 0; ks < 8; ks += 2) { a0 = MFMA32(xa[ks], wb[ks], a0); a1 = MFMA32(xa[ks + 1], wb[ks + 1], a1); }
      return a0[0] + a1[0];
    };
    if (cw < 2) {
      wait_cnt(L, C_WQ);
      STAMP(2);
      const float qp = proj();
      if (lane < 16)
        __hip_atomic_store((GAS u64*)(ws + W_QG + (size_t)(q_rh * 4096 + q_col0 + 16 * cw + lane) * 8),
                           ((u64)tag << 32) | __float_as_uint(qp), __ATOMIC_RELAXED, __HIP_MEMORY_SCOPE_AGENT);
      STAMP(3);
    }
    wait_cnt(L, C_K);
    STAMP(4);
    {
      bf16x8 kb[8][4];
#pragma unroll
      for (int kt = 0; kt < 8; ++kt)
#pragma unroll
        for (int ks = 0; ks < 4; ++ks) kb[kt][ks] = *(LAS bf16x8*)(L + L_K + kt * 4096 + c * 256 + (((4 * ks + g) ^ c) * 16));
      SCHED();
      float qlo[2], qhi[2];
      gather<2, SLP>(wsr, W_QG + ((4 * h + cw) * 128 + 2 * lane) * 8, 32768, tag, qlo, qhi);
      STAMP(5);
      *(LAS unsigned*)(L + L_QA + cw * 256 + lane * 4) = pack_bf(qlo[0] + qlo[1], qhi[0] + qhi[1]);
      bf16x8 qa[4];
#pragma unroll
      for (int ks = 0; ks < 4; ++ks) qa[ks] = *(LAS bf16x8*)(L + L_QA + cw * 256 + ks * 64 + g * 16);
      SCHED();
      f32x4 sc[8];
#pragma unroll
      for (int kt = 0; kt < 8; ++kt) sc[kt] = MFMA32(qa[0], kb[kt][0], (f32x4{0.f, 0.f, 0.f, 0.f}));
#pragma unroll
      for (int ks = 1; ks < 4; ++ks)
#pragma unroll
        for (int kt = 0; kt < 8; ++kt) sc[kt] = MFMA32(qa[ks], kb[kt][ks], sc[kt]);
      float s0 = sel4(g, sc[0][0], sc[2][0], sc[4][0], sc[6][0]);
      float s1 = sel4(g, sc[1][0], sc[3][0], sc[5][0], sc[7][0]);
      unsigned r = pack_bf(s0, s1);
      r = pack_bf(bf_lo(r) * 0.08838834764831845f, bf_hi(r) * 0.08838834764831845f);
      s0 = bf_lo(r); s1 = bf_hi(r);
      const float m = wave_max(fmaxf(s0, s1));
      const float p0 = __builtin_amdgcn_exp2f((s0 - m) * LOG2E), p1 = __builtin_amdgcn_exp2f((s1 - m) * LOG2E);
      const unsigned pb = pack_bf(p0, p1);
      *(LAS u16*)(L + L_PB + cw * 320 + (32 * g + c) * 2) = (u16)pb;
      *(LAS u16*)(L + L_PB + cw * 320 + (32 * g + 16 + c) * 2) = (u16)(pb >> 16);
      const float l = wave_sum(p0 + p1);
      LDS_WAIT();
      lds_flag(F_PF + 4 * cw, 1);
      if (lane == 0) {
        const u32x4 ml = {__float_as_uint(m), tag, __float_as_uint(l), tag};
        __builtin_amdgcn_raw_buffer_store_b128(ml, wsr, W_ML + ((4 * h + cw) * 32 + i) * 16, 0, SC1);
      }
    }
    STAMP(6);
    if (cw >= 2) {
      wait_cnt(L, C_GU);
      const float up = proj();
      if (lane < 16 && i < 28)
        __hip_atomic_store((GAS u64*)(ws + W_HG + (size_t)((u_mat * 2 + u_rh) * DF + u_col0 + 16 * (cw - 2) + lane) * 8),
                           ((u64)tag << 32) | __float_as_uint(up), __ATOMIC_RELAXED, __HIP_MEMORY_SCOPE_AGENT);
      STAMP(7);
    }
    {
      wait_flag4(L, F_PF);
      bf16x8 pa[4];
#pragma unroll
      for (int ks = 0; ks < 4; ++ks) pa[ks] = *(LAS bf16x8*)(L + L_PB + (c >> 2) * 320 + ks * 64 + g * 16);
      const int f = q4 | ((g & 1) << 2);
      const int vb0 = L_V + (8 * g + q4) * 256 + (((2 * cw) ^ f) * 32) + pp * 8;
      const int vb1 = L_V + (8 * g + q4) * 256 + (((2 * cw + 1) ^ f) * 32) + pp * 8;
      f32x4 o0 = {0.f, 0.f, 0.f, 0.f}, o1 = {0.f, 0.f, 0.f, 0.f};
#pragma unroll
      for (int ks = 0; ks < 4; ++ks) {
        wait_cnt(L, C_V0 + 4 * ks);
        const bf16x8 b0 = tr_pair(L, vb0 + ks * 8192, vb0 + ks * 8192 + 1024);
        const bf16x8 b1 = tr_pair(L, vb1 + ks * 8192, vb1 + ks * 8192 + 1024);
        SCHED();
        o0 = MFMA32(pa[ks], b0, o0);
        o1 = MFMA32(pa[ks], b1, o1);
      }
      const u32x4 po = {__float_as_uint(o0[0]), tag, __float_as_uint(o1[0]), tag};
      __builtin_amdgcn_raw_buffer_store_b128(po, wsr, W_PO + (((4 * h + g) * 32 + i) * 128 + 32 * cw + 2 * c) * 8, 0, SC1);
      lds_flag(F_POF + 4 * cw, 1);
    }
    STAMP(9);
    {
      const int f = q4 | ((g & 1) << 2), fd = (4 * g + q4) & 7;
      bf16x8 wb[4];
      s16x4 db[4];
      wait_cnt(L, C_WO);
#pragma unroll
      for (int u = 0; u < 4; ++u) {
        const int a = L_WO + (8 * g + q4) * 512 + (((4 * cw + u) ^ f) * 32) + pp * 8;
        wb[u] = tr_pair(L, a, a + 2048);
      }
      wait_cnt(L, C_DN);
      STAMP(10);
#pragma unroll
      for (int u = 0; u < 4; ++u)
        db[u] = __builtin_amdgcn_ds_read_tr16_b64_v4i16((LAS s16x4*)(L + L_DN + (4 * g + q4) * 512 + (((4 * cw + u) ^ fd) * 32) + pp * 8));
      wait_flag1(L, F_HAF);
      const s16x4 ha = *(LAS s16x4*)(L + L_HA + g * 8);
      f32x4 dd[4], ss[4];
#pragma unroll
      for (int u = 0; u < 4; ++u) dd[u] = __builtin_amdgcn_mfma_f32_16x16x16bf16_1k(ha, db[u], f32x4{0.f, 0.f, 0.f, 0.f}, 0, 0, 0);
      const float dsel = sel4(g, dd[0][0], dd[1][0], dd[2][0], dd[3][0]);
      SCHED();
      wait_flag1(L, F_OAF);
      const bf16x8 oa = *(LAS bf16x8*)(L + L_OA + g * 16);
#pragma unroll
      for (int u = 0; u < 4; ++u) ss[u] = MFMA32(oa, wb[u], (f32x4{0.f, 0.f, 0.f, 0.f}));
      const float ssel = sel4(g, ss[0][0], ss[1][0], ss[2][0], ss[3][0]);
      const u32x4 o3 = {__float_as_uint(ssel), tag, __float_as_uint(dsel), tag};
      __builtin_amdgcn_raw_buffer_store_b128(o3, wsr, W_O3 + (rg * 512 + ch * 256 + cw * 64 + lane) * 16, 0, SC1);
      lds_flag(F_O3F + 4 * cw, 1);
    }
    STAMP(12);
    return;
  }

  if (wv == 6) {
    const int col0 = (h * 8 + (i >> 2)) * 8 + 2 * (i & 3);
    const unsigned xw = __builtin_amdgcn_readlane(xv[i & 3], col0 >> 3);
    const unsigned tag = __builtin_amdgcn_readfirstlane(e_in) * 1664525u + 1013904223u;
    const int r = rg >> 2, dc = rg & 3;
    wait_flag4(L, F_PF);
    wait_cnt(L, C_V0 + 4);
    STAMP(1);
    float w[8];
    {
      float m[1], l[1];
      gather<1, SLP>(wsr, W_ML + (r * 32 + (lane & 31)) * 16, 0, tag, m, l);
      const float M = wave_max(m[0]);
      const float e = __builtin_amdgcn_exp2f((m[0] - M) * LOG2E);
      const float Lsum = wave_sum(e * l[0]) * 0.5f;
      *(LAS float*)(L + L_WS + ((lane & 3) * 8 + ((lane & 31) >> 2)) * 4) = e * rcp(Lsum);
      const f32x4 w0 = *(LAS f32x4*)(L + L_WS + g * 32), w1 = *(LAS f32x4*)(L + L_WS + g * 32 + 16);
#pragma unroll
      for (int k = 0; k < 4; ++k) { w[k] = w0[k]; w[4 + k] = w1[k]; }
    }
    STAMP(2);
    wait_flag4(L, F_POF);
    STAMP(3);
    {
      float v0[8], v1[8];
      gather<8, 0>(wsr, W_PO + ((r * 32 + g) * 128 + 32 * dc + 2 * c) * 8, 4096, tag, v0, v1);
      STAMP(4);
      float a0 = 0.f, a1 = 0.f;
#pragma unroll
      for (int k = 0; k < 8; ++k) { a0 = fmaf(w[k], v0[k], a0); a1 = fmaf(w[k], v1[k], a1); }
      a0 = xrow_sum(a0); a1 = xrow_sum(a1);
      const unsigned ob = pack_bf(a0, a1);
      *(LAS u16*)(L + L_OA + c * 2) = (u16)ob;
      *(LAS u16*)(L + L_OA + 32 + c * 2) = (u16)(ob >> 16);
      LDS_WAIT();
      lds_flag(F_OAF, 1);
    }
    wait_flag4(L, F_O3F);
    STAMP(6);
    {
      float vs[4], vd[4];
      gather<4, 0>(wsr, W_O3 + ((lane >> 1) * 512 + col0 + (lane & 1)) * 16, 32 * 8192, tag, vs, vd);
      STAMP(7);
      float s = (vs[0] + vs[1]) + (vs[2] + vs[3]);
      float d = (vd[0] + vd[1]) + (vd[2] + vd[3]);
      s += dpp<XOR2>(s); d += dpp<XOR2>(d);
      s += dpp<ROR4>(s); d += dpp<ROR4>(d);
      s += dpp<ROR8>(s); d += dpp<ROR8>(d);
      s = xrow_sum(s); d = xrow_sum(d);
      const unsigned sd = pack_bf(s, d);
      s = bf_lo(sd); d = bf_hi(sd);
      const float gt = rbf(d * rcp(1.f + __builtin_amdgcn_exp2f(-d * LOG2E)));
      const float xe = (lane & 1) ? bf_hi(xw) : bf_lo(xw);
      const unsigned res = pack_bf(xe + rbf(s * gt), 0.f);
      if (lane < 2) *(GAS u16*)(out + col0 + lane) = (u16)res;
      if (b == 0 && lane == 0) __hip_atomic_store((GAS unsigned*)(ws + W_E), tag, __ATOMIC_RELAXED, __HIP_MEMORY_SCOPE_AGENT);
    }
    STAMP(8);
    {
      for (int z = 0; z < 8; ++z) __builtin_amdgcn_s_sleep(64);
      LDS_WAIT();
      unsigned t0 = *(volatile LAS unsigned*)(L + L_END + (DIAG_WV * 16 + DIAG_IDX0) * 4);
      unsigned t1 = *(volatile LAS unsigned*)(L + L_END + (DIAG_WV * 16 + DIAG_IDX) * 4);
      unsigned val = __builtin_amdgcn_readfirstlane(t1 - t0);
      if (val > 5000u) val = 5000u;
      const unsigned s0 = (unsigned)__builtin_amdgcn_s_memrealtime();
      while ((unsigned)__builtin_amdgcn_s_memrealtime() - s0 < val * DIAG_K) __builtin_amdgcn_s_sleep(8);
    }
    return;
  }

  {
    STAMP(4);
    if (WBL2 == 1 || i == 0) {
      __builtin_amdgcn_fence(__ATOMIC_RELEASE, "agent");
      asm volatile("s_waitcnt vmcnt(0)" ::: "memory");
    }
    STAMP(5);
    const unsigned tag = __builtin_amdgcn_readfirstlane(e_in) * 1664525u + 1013904223u;
    wait_cnt(L, C_V0 + 8);
    STAMP(1);
    const GAS u64* src = (const GAS u64*)(ws + W_HG + (size_t)(g * DF + rg * 14 + min(c, 13)) * 8);
    u64 v;
#pragma nounroll
    for (unsigned n = 0; n < SPIN_GLB; ++n) {
      v = __hip_atomic_load(src, __ATOMIC_RELAXED, __HIP_MEMORY_SCOPE_AGENT);
      if (none((unsigned)(v >> 32) != tag)) break;
      __builtin_amdgcn_s_sleep(SLP);
    }
    STAMP(2);
    auto s = __builtin_amdgcn_permlane16_swap((unsigned)v, (unsigned)v, false, false);
    const float y = __uint_as_float(s[0]) + __uint_as_float(s[1]);
    auto t = __builtin_amdgcn_permlane32_swap(__float_as_uint(y), __float_as_uint(y), false, false);
    const unsigned gu = pack_bf(__uint_as_float(t[0]), __uint_as_float(t[1]));
    const float gg = bf_lo(gu), uu = bf_hi(gu);
    const float z = 0.7978845608028654f * (gg + 0.044715f * gg * gg * gg);
    const float ge = rbf(gg * rcp(1.f + __builtin_amdgcn_exp2f(-2.f * LOG2E * z)));
    const unsigned hb = c < 14 ? pack_bf(ge * uu, 0.f) : 0u;
    *(LAS u16*)(L + L_HA + c * 2) = (u16)hb;
    LDS_WAIT();
    lds_flag(F_HAF, 1);
    STAMP(3);
  }
  return;
  }

  {
    const int lw = wv;
    const int cnt_addr = F_CNT + 4 * lw;
    const size_t hoff = ((size_t)h * SK + (SK - WIN)) * 256;
    {
      const int row = lane >> 2, chunk = (lane & 3) ^ (((row >> 3) & 1) << 1);
      const char* src = (const char*)wq + (size_t)(q_rh * 256 + lw * 16) * 8192 + q_col0 * 2;
      const unsigned vo = row * 8192 + chunk * 16;
      issue_run<0, 8>([&](auto J) {
        constexpr int j = decltype(J)::value;
        DMA(src + (size_t)j * 32 * 8192 + vo, L + L_WQ + (2 * j + lw) * 1024);
        after_issue<j + 1>(cnt_addr);
      });
    }
    __builtin_amdgcn_s_barrier();
    {
      const char* src = (const char*)kc + hoff + lw * 1024;
      const unsigned vo0 = g * 256 + ((c ^ (4 * lw + g)) * 16), vo1 = g * 256 + ((c ^ (4 * (lw + 2) + g)) * 16) + 2048;
      issue_run<0, 16>([&](auto J) {
        constexpr int j = decltype(J)::value, blk = j >> 1;
        const char* s = src + (size_t)(blk * 32 + (i ^ ((blk & 1) << 1))) * 4096;
        DMA(s + ((j & 1) ? vo1 : vo0), L + L_K + (2 * j + lw) * 1024);
        after_issue<8 + j + 1>(cnt_addr);
      });
    }
    {
      const int row = lane >> 2, chunk = (lane & 3) ^ (((row >> 3) & 1) << 1);
      const char* src = (const char*)(u_mat ? wu : wg) + (size_t)(u_rh * 256 + lw * 16) * (DF * 2) + u_col0 * 2;
      const unsigned vo = row * (DF * 2) + chunk * 16;
      issue_run<0, 8>([&](auto J) {
        constexpr int j = decltype(J)::value;
        DMA(src + (size_t)j * 32 * (DF * 2) + vo, L + L_GU + (2 * j + lw) * 1024);
        after_issue<24 + j + 1>(cnt_addr);
      });
    }
    {
      const char* src = (const char*)vc + hoff + lw * 1024;
      const unsigned vo0 = g * 256 + ((c ^ (g << 1)) * 16), vo1 = g * 256 + ((c ^ ((g | 4) << 1)) * 16) + 2048;
      issue_run<0, 16>([&](auto J) {
        constexpr int j = decltype(J)::value, blk = j >> 1;
        const char* s = src + (size_t)(blk * 32 + (i ^ ((blk & 1) << 1))) * 4096;
        DMA(s + ((j & 1) ? vo1 : vo0), L + L_V + (2 * j + lw) * 1024);
        after_issue<32 + j + 1>(cnt_addr);
      });
    }
    {
      const int r2 = lane >> 5, pos = lane & 31;
      const char* src = (const char*)wo + (size_t)(rg * 32 + 2 * lw) * 1024 + ch * 512;
      const unsigned vo0 = r2 * 1024 + ((pos ^ ((2 * lw + r2) << 1)) * 16), vo1 = r2 * 1024 + ((pos ^ (((2 * lw + r2) | 4) << 1)) * 16);
      issue_run<0, 8>([&](auto J) {
        constexpr int j = decltype(J)::value;
        DMA(src + j * 4096 + (((j >> 1) & 1) ? vo1 : vo0), L + L_WO + (2 * j + lw) * 1024);
        after_issue<48 + j + 1>(cnt_addr);
      });
      const char* srd = (const char*)wd + (size_t)(rg * 14 + 2 * lw) * 1024 + ch * 512;
      issue_run<0, 4>([&](auto J) {
        constexpr int j = decltype(J)::value;
        const int back = (j == 3) ? lw * 2048 : 0;
        DMA(srd + j * 4096 - back + ((j & 1) ? vo1 : vo0), L + L_DN + (2 * j + lw) * 1024);
        after_issue<56 + j + 1>(cnt_addr);
      });
    }
    drain_to<C_WQ>(cnt_addr); drain_to<C_K>(cnt_addr); drain_to<C_GU>(cnt_addr);
    drain_to<36>(cnt_addr); drain_to<40>(cnt_addr); drain_to<44>(cnt_addr); drain_to<48>(cnt_addr);
    drain_to<C_WO>(cnt_addr); drain_to<C_DN>(cnt_addr);
    STAMP(2);
  }
}

__global__ void k_nop() {}
extern "C" void kernel_launch(void* const* d_in, const int* in_sizes, int n_in, void* d_out, int out_size, void* d_ws,
                              size_t ws_size, hipStream_t stream) {
  (void)in_sizes; (void)n_in; (void)out_size; (void)ws_size;
  (void)hipFuncSetAttribute((const void*)fused_decode, hipFuncAttributeMaxDynamicSharedMemorySize, L_END + 512);
  fused_decode<<<256, 512, L_END + 512, stream>>>((const u16*)d_in[0], (const u16*)d_in[1], (const u16*)d_in[2], (const u16*)d_in[3],
                                            (const u16*)d_in[4], (const u16*)d_in[6], (const u16*)d_in[7], (const u16*)d_in[8],
                                            (u16*)d_out, (char*)d_ws + WS_OFF(ws_size));
}
```

```cpp
#define DIAG_WV 6
#define DIAG_IDX 0
#define DIAG_IDX0 0
#define DIAG_K 0
#define DIAG_CLK 0
#define POST_EMPTY 1
#define POST_K 0
#include <hip/hip_runtime.h>
#include <hip/hip_bf16.h>
#include <stdint.h>
#include <type_traits>

typedef unsigned short u16;
typedef unsigned long long u64;
typedef unsigned u32x4 __attribute__((ext_vector_type(4)));
typedef unsigned u32x2 __attribute__((ext_vector_type(2)));
typedef float f32x4 __attribute__((ext_vector_type(4)));
typedef float f32x2 __attribute__((ext_vector_type(2)));
typedef short s16x4 __attribute__((ext_vector_type(4)));
typedef short s16x8 __attribute__((ext_vector_type(8)));
typedef __bf16 bf16x8 __attribute__((ext_vector_type(8)));
typedef __bf16 bf16x2 __attribute__((ext_vector_type(2)));
#define LAS __attribute__((address_space(3)))
#define GAS __attribute__((address_space(1)))

#define THR 10
#define STAMPS 0
#define CODE_KB 0
#define LSLP 0
#define DBG 0
#define WS_OFF(s) 0
#define SLP 2

constexpr int SK = 65536, WIN = 4096, DF = 1792;
constexpr int L_WQ = 0, L_K = 16384, L_GU = 49152, L_V = 65536, L_WO = 98304, L_DN = 114688, L_MISC = 122880;
constexpr int L_XN = L_MISC, L_QA = L_XN + 1024, L_PB = L_QA + 1024, L_WS = L_PB + 1280, L_OA = L_WS + 128, L_HA = L_OA + 64,
              L_FL = L_HA + 64, L_END = L_FL + 128;
constexpr int F_CNT = L_FL, F_PF = L_FL + 16, F_POF = L_FL + 32, F_O3F = L_FL + 48, F_OAF = L_FL + 64, F_HAF = L_FL + 68;
constexpr int W_E = 0, W_QG = 4096, W_HG = W_QG + 65536, W_ML = W_HG + 65536, W_PO = W_ML + 16384 + 4096 * 3,
              W_O3 = W_PO + (1 << 20), W_END = W_O3 + (1 << 20);
constexpr int W_ST = 8 << 20;
constexpr int C_WQ = 8, C_K = 24, C_GU = 32, C_V0 = 36, C_WO = 56, C_DN = 60, C_ALL = 60;
constexpr unsigned SPIN_LDS = 1u << 22, SPIN_GLB = 1u << 17;
constexpr float LOG2E = 1.4426950408889634f;
constexpr int OOB = 0x7ffffff0;

__device__ __forceinline__ float bf_lo(unsigned w) { return __uint_as_float(w << 16); }
__device__ __forceinline__ float bf_hi(unsigned w) { return __uint_as_float(w & 0xffff0000u); }
__device__ __forceinline__ unsigned pack_bf(float lo, float hi) {
  f32x2 v = {lo, hi};
  return __builtin_bit_cast(unsigned, __builtin_convertvector(v, bf16x2));
}
__device__ __forceinline__ float rbf(float f) { return bf_lo(pack_bf(f, f)); }
__device__ __forceinline__ float rcp(float f) { return __builtin_amdgcn_rcpf(f); }
__device__ __forceinline__ float vmax(float a, float b) { float r; asm("v_max_f32 %0, %1, %2" : "=v"(r) : "v"(a), "v"(b)); return r; }
template <int CTRL> __device__ __forceinline__ float dpp(float x) {
  return __builtin_bit_cast(float, __builtin_amdgcn_mov_dpp(__builtin_bit_cast(int, x), CTRL, 0xf, 0xf, true));
}
constexpr int XOR1 = 0xB1, XOR2 = 0x4E, ROR4 = 0x124, ROR8 = 0x128;
__device__ __forceinline__ float xrow_sum(float x) {
  auto s = __builtin_amdgcn_permlane16_swap(__float_as_uint(x), __float_as_uint(x), false, false);
  x = __uint_as_float(s[0]) + __uint_as_float(s[1]);
  auto t = __builtin_amdgcn_permlane32_swap(__float_as_uint(x), __float_as_uint(x), false, false);
  return __uint_as_float(t[0]) + __uint_as_float(t[1]);
}
__device__ __forceinline__ float xrow_max(float x) {
  auto s = __builtin_amdgcn_permlane16_swap(__float_as_uint(x), __float_as_uint(x), false, false);
  x = fmaxf(__uint_as_float(s[0]), __uint_as_float(s[1]));
  auto t = __builtin_amdgcn_permlane32_swap(__float_as_uint(x), __float_as_uint(x), false, false);
  return fmaxf(__uint_as_float(t[0]), __uint_as_float(t[1]));
}
__device__ __forceinline__ float wave_sum(float x) {
  x += dpp<XOR1>(x); x += dpp<XOR2>(x); x += dpp<ROR4>(x); x += dpp<ROR8>(x);
  return xrow_sum(x);
}
__device__ __forceinline__ float wave_max(float x) {
  x = fmaxf(x, dpp<XOR1>(x)); x = fmaxf(x, dpp<XOR2>(x)); x = fmaxf(x, dpp<ROR4>(x)); x = fmaxf(x, dpp<ROR8>(x));
  return xrow_max(x);
}
__device__ __forceinline__ float sel4(int g, float a0, float a1, float a2, float a3) {
  const float lo = (g & 1) ? a1 : a0, hi = (g & 1) ? a3 : a2;
  return (g & 2) ? hi : lo;
}
__device__ __forceinline__ bool none(bool bad) { return __builtin_amdgcn_ballot_w64(bad) == 0ull; }
#define SCHED() __builtin_amdgcn_sched_barrier(0)
#define MEMBAR() asm volatile("" ::: "memory")
#define LDS_WAIT() asm volatile("s_waitcnt lgkmcnt(0)" ::: "memory")
__device__ __forceinline__ void lds_flag(int addr, unsigned v) {
  asm volatile("ds_write_b32 %0, %1" ::"v"(addr), "v"(v) : "memory");
}
__device__ __forceinline__ void wait_cnt(LAS char* L, unsigned thr) {
#pragma nounroll
  for (unsigned n = 0; n < SPIN_LDS; ++n) {
    u32x2 c = *(volatile LAS u32x2*)(L + F_CNT);
    if (__builtin_amdgcn_readfirstlane(min(c[0], c[1])) >= thr) break;
    if (LSLP) __builtin_amdgcn_s_sleep(LSLP);
  }
  MEMBAR();
}
__device__ __forceinline__ void wait_flag4(LAS char* L, int off) {
#pragma nounroll
  for (unsigned n = 0; n < SPIN_LDS; ++n) {
    u32x4 c = *(volatile LAS u32x4*)(L + off);
    if (__builtin_amdgcn_readfirstlane(c[0] & c[1] & c[2] & c[3])) break;
    if (LSLP) __builtin_amdgcn_s_sleep(LSLP);
  }
  MEMBAR();
}
__device__ __forceinline__ void wait_flag1(LAS char* L, int off) {
#pragma nounroll
  for (unsigned n = 0; n < SPIN_LDS; ++n) {
    unsigned c = *(volatile LAS unsigned*)(L + off);
    if (__builtin_amdgcn_readfirstlane(c)) break;
    if (LSLP) __builtin_amdgcn_s_sleep(LSLP);
  }
  MEMBAR();
}
__device__ __forceinline__ bf16x8 tr_pair(LAS char* L, int a0, int a1) {
  s16x4 t0 = __builtin_amdgcn_ds_read_tr16_b64_v4i16((LAS s16x4*)(L + a0));
  s16x4 t1 = __builtin_amdgcn_ds_read_tr16_b64_v4i16((LAS s16x4*)(L + a1));
  s16x8 b = {t0[0], t0[1], t0[2], t0[3], t1[0], t1[1], t1[2], t1[3]};
  return __builtin_bit_cast(bf16x8, b);
}
#define MFMA32(a, b, c) __builtin_amdgcn_mfma_f32_16x16x32_bf16((a), (b), (c), 0, 0, 0)
#define DMA(src, dst) __builtin_amdgcn_global_load_lds((const unsigned*)(src), (LAS unsigned*)(dst), 16, 0, 2)
#define SC1 16
#define LD4(off, soff) __builtin_bit_cast(u32x4, __builtin_amdgcn_raw_buffer_load_b128(wsr, (off), (soff), SC1))

template <int N, int SLEEP>
__device__ __forceinline__ void gather(__amdgpu_buffer_rsrc_t wsr, int off, int stride, unsigned tag, float (&lo)[N], float (&hi)[N]) {
  int o[N];
#pragma unroll
  for (int k = 0; k < N; ++k) { o[k] = off; lo[k] = 0.f; hi[k] = 0.f; }
#pragma nounroll
  for (unsigned n = 0; n < SPIN_GLB; ++n) {
    MEMBAR();
    u32x4 v[N];
#pragma unroll
    for (int k = 0; k < N; ++k) v[k] = LD4(o[k], k * stride);
    bool pend = false;
#pragma unroll
    for (int k = 0; k < N; ++k) {
      const bool ok = (v[k][1] == tag) & (v[k][3] == tag) & (o[k] != OOB);
      lo[k] = ok ? __uint_as_float(v[k][0]) : lo[k];
      hi[k] = ok ? __uint_as_float(v[k][2]) : hi[k];
      o[k] = ok ? OOB : o[k];
      pend |= o[k] != OOB;
    }
    if (none(pend)) break;
    if (SLEEP) __builtin_amdgcn_s_sleep(SLEEP);
  }
}

#define STAMP(k) do { unsigned t_ = (unsigned)__builtin_amdgcn_s_memrealtime(); lds_flag(L_END + (wv * 16 + (k)) * 4, t_); } while (0)

template <int J> __device__ __forceinline__ void after_issue(int cnt_addr) {
  if constexpr (J > THR) {
    asm volatile("s_waitcnt vmcnt(%0)" ::"n"(THR) : "memory");
    constexpr int LANDED = J - THR;
    if constexpr (LANDED == C_WQ || LANDED == C_K || LANDED == C_GU || (LANDED >= C_V0 && LANDED <= 48 && (LANDED & 3) == 0) ||
                  LANDED == C_WO)
      lds_flag(cnt_addr, LANDED);
  }
}
template <int LANDED> __device__ __forceinline__ void drain_to(int cnt_addr) {
  if constexpr (LANDED > C_ALL - THR) {
    asm volatile("s_waitcnt vmcnt(%0)" ::"n"(C_ALL - LANDED) : "memory");
    lds_flag(cnt_addr, LANDED);
  }
}
template <int J0, int N, typename F> __device__ __forceinline__ void issue_run(F f) {
  if constexpr (N > 0) {
    f(std::integral_constant<int, J0>{});
    issue_run<J0 + 1, N - 1>(f);
  }
}

__global__ void __launch_bounds__(512, 2) __attribute__((amdgpu_waves_per_eu(2, 2)))
fused_decode(const u16* __restrict__ x, const u16* __restrict__ kc, const u16* __restrict__ vc, const u16* __restrict__ wq,
             const u16* __restrict__ wo, const u16* __restrict__ wg, const u16* __restrict__ wu, const u16* __restrict__ wd,
             u16* __restrict__ out, char* ws) {
  extern __shared__ __attribute__((aligned(16))) char lds_raw[];
  LAS char* L = (LAS char*)lds_raw;
  const int b = blockIdx.x, tid = threadIdx.x, lane = tid & 63;
  const int wv = __builtin_amdgcn_readfirstlane(tid >> 6);
  const int h = b & 7, i = b >> 3;
  const int g = lane >> 4, c = lane & 15;
  const int q_rh = i >> 4, q_col0 = 512 * h + 32 * (i & 15);
  const int gi = i < 28 ? i : i - 4;
  const int combo = (gi >> 1) * 8 + h;
  const int u_mat = (combo >> 1) & 1, u_rh = combo & 1, u_col0 = (combo >> 2) * 64 + (gi & 1) * 32;
  const int rg = h * 16 + (i >> 1), ch = i & 1;
  STAMP(0);

  if (wv >= 2) {
  if (wv == 7 && lane < 32) *(LAS unsigned*)(L + L_FL + lane * 4) = 0u;
  __builtin_amdgcn_s_setprio(2);
  const __amdgpu_buffer_rsrc_t wsr = __builtin_amdgcn_make_buffer_rsrc((void*)ws, 0, W_END, 0x00020000);
  const u32x4 xv = *(const GAS u32x4*)(x + lane * 8);
  const unsigned e_in = __hip_atomic_load((GAS unsigned*)(ws + W_E), __ATOMIC_RELAXED, __HIP_MEMORY_SCOPE_AGENT);
  SCHED();
  LDS_WAIT();
  __builtin_amdgcn_s_barrier();
  SCHED();

  if (wv < 6) {
    const int cw = wv - 2;
    {
      float f[8];
#pragma unroll
      for (int k = 0; k < 4; ++k) { f[2 * k] = bf_lo(xv[k]); f[2 * k + 1] = bf_hi(xv[k]); }
      float s = ((f[0] + f[1]) + (f[2] + f[3])) + ((f[4] + f[5]) + (f[6] + f[7]));
      const float mean = wave_sum(s) * (1.f / 512.f);
      float v0 = 0.f, v1 = 0.f;
#pragma unroll
      for (int k = 0; k < 8; k += 2) { const float d0 = f[k] - mean, d1 = f[k + 1] - mean; v0 = fmaf(d0, d0, v0); v1 = fmaf(d1, d1, v1); }
      const float var = wave_sum(v0 + v1) * (1.f / 512.f);
      const float mb = rbf(mean);
      const float den = rbf(__builtin_amdgcn_sqrtf(rbf(rbf(var) + 1e-5f)));
      const float inv = rcp(den);
      u32x4 xn;
#pragma unroll
      for (int k = 0; k < 4; ++k) {
        const unsigned d = pack_bf(f[2 * k] - mb, f[2 * k + 1] - mb);
        float a0 = bf_lo(d), a1 = bf_hi(d);
        float q0 = a0 * inv, q1 = a1 * inv;
        q0 = fmaf(fmaf(-q0, den, a0), inv, q0); q1 = fmaf(fmaf(-q1, den, a1), inv, q1);
        xn[k] = pack_bf(q0, q1);
      }
      *(LAS u32x4*)(L + L_XN + lane * 16) = xn;
    }
    const unsigned tag = __builtin_amdgcn_readfirstlane(e_in) * 1664525u + 1013904223u;
    STAMP(1);
    const int q4 = (lane & 15) >> 2, pp = lane & 3;
    const int p_tile = cw < 2 ? L_WQ : L_GU, p_rh = cw < 2 ? q_rh : u_rh;
    bf16x8 xa[8];
#pragma unroll
    for (int ks = 0; ks < 8; ++ks) xa[ks] = *(LAS bf16x8*)(L + L_XN + p_rh * 512 + g * 16 + ks * 64);
    auto proj = [&]() -> float {
      const int tb = p_tile + (8 * g + q4) * 64 + (((2 * (cw & 1) + (pp >> 1)) ^ ((g & 1) << 1)) * 16) + 8 * (pp & 1);
      bf16x8 wb[8];
#pragma unroll
      for (int ks = 0; ks < 8; ++ks) wb[ks] = tr_pair(L, tb + ks * 2048, tb + ks * 2048 + 256);
      SCHED();
      f32x4 a0 = {0.f, 0.f, 0.f, 0.f}, a1 = {0.f, 0.f, 0.f, 0.f};
#pragma unroll
      for (int ks = 0; ks < 8; ks += 2) { a0 = MFMA32(xa[ks], wb[ks], a0); a1 = MFMA32(xa[ks + 1], wb[ks + 1], a1); }
      return a0[0] + a1[0];
    };
    if (cw < 2) {
      wait_cnt(L, C_WQ);
      STAMP(2);
      const float qp = proj();
      if (lane < 16)
        __hip_atomic_store((GAS u64*)(ws + W_QG + (size_t)(q_rh * 4096 + q_col0 + 16 * cw + lane) * 8),
                           ((u64)tag << 32) | __float_as_uint(qp), __ATOMIC_RELAXED, __HIP_MEMORY_SCOPE_AGENT);
      STAMP(3);
    }
    wait_cnt(L, C_K);
    STAMP(4);
    {
      bf16x8 kb[8][4];
#pragma unroll
      for (int kt = 0; kt < 8; ++kt)
#pragma unroll
        for (int ks = 0; ks < 4; ++ks) kb[kt][ks] = *(LAS bf16x8*)(L + L_K + kt * 4096 + c * 256 + (((4 * ks + g) ^ c) * 16));
      SCHED();
      float qlo[2], qhi[2];
      gather<2, SLP>(wsr, W_QG + ((4 * h + cw) * 128 + 2 * lane) * 8, 32768, tag, qlo, qhi);
      STAMP(5);
      *(LAS unsigned*)(L + L_QA + cw * 256 + lane * 4) = pack_bf(qlo[0] + qlo[1], qhi[0] + qhi[1]);
      bf16x8 qa[4];
#pragma unroll
      for (int ks = 0; ks < 4; ++ks) qa[ks] = *(LAS bf16x8*)(L + L_QA + cw * 256 + ks * 64 + g * 16);
      SCHED();
      f32x4 sc[8];
#pragma unroll
      for (int kt = 0; kt < 8; ++kt) sc[kt] = MFMA32(qa[0], kb[kt][0], (f32x4{0.f, 0.f, 0.f, 0.f}));
#pragma unroll
      for (int ks = 1; ks < 4; ++ks)
#pragma unroll
        for (int kt = 0; kt < 8; ++kt) sc[kt] = MFMA32(qa[ks], kb[kt][ks], sc[kt]);
      float s0 = sel4(g, sc[0][0], sc[2][0], sc[4][0], sc[6][0]);
      float s1 = sel4(g, sc[1][0], sc[3][0], sc[5][0], sc[7][0]);
      unsigned r = pack_bf(s0, s1);
      r = pack_bf(bf_lo(r) * 0.08838834764831845f, bf_hi(r) * 0.08838834764831845f);
      s0 = bf_lo(r); s1 = bf_hi(r);
      const float m = wave_max(fmaxf(s0, s1));
      const float p0 = __builtin_amdgcn_exp2f((s0 - m) * LOG2E), p1 = __builtin_amdgcn_exp2f((s1 - m) * LOG2E);
      const unsigned pb = pack_bf(p0, p1);
      *(LAS u16*)(L + L_PB + cw * 320 + (32 * g + c) * 2) = (u16)pb;
      *(LAS u16*)(L + L_PB + cw * 320 + (32 * g + 16 + c) * 2) = (u16)(pb >> 16);
      const float l = wave_sum(p0 + p1);
      LDS_WAIT();
      lds_flag(F_PF + 4 * cw, 1);
      if (lane == 0) {
        const u32x4 ml = {__float_as_uint(m), tag, __float_as_uint(l), tag};
        __builtin_amdgcn_raw_buffer_store_b128(ml, wsr, W_ML + ((4 * h + cw) * 32 + i) * 16, 0, SC1);
      }
    }
    STAMP(6);
    if (cw >= 2) {
      wait_cnt(L, C_GU);
      const float up = proj();
      if (lane < 16 && i < 28)
        __hip_atomic_store((GAS u64*)(ws + W_HG + (size_t)((u_mat * 2 + u_rh) * DF + u_col0 + 16 * (cw - 2) + lane) * 8),
                           ((u64)tag << 32) | __float_as_uint(up), __ATOMIC_RELAXED, __HIP_MEMORY_SCOPE_AGENT);
      STAMP(7);
    }
    {
      wait_flag4(L, F_PF);
      bf16x8 pa[4];
#pragma unroll
      for (int ks = 0; ks < 4; ++ks) pa[ks] = *(LAS bf16x8*)(L + L_PB + (c >> 2) * 320 + ks * 64 + g * 16);
      const int f = q4 | ((g & 1) << 2);
      const int vb0 = L_V + (8 * g + q4) * 256 + (((2 * cw) ^ f) * 32) + pp * 8;
      const int vb1 = L_V + (8 * g + q4) * 256 + (((2 * cw + 1) ^ f) * 32) + pp * 8;
      f32x4 o0 = {0.f, 0.f, 0.f, 0.f}, o1 = {0.f, 0.f, 0.f, 0.f};
#pragma unroll
      for (int ks = 0; ks < 4; ++ks) {
        wait_cnt(L, C_V0 + 4 * ks);
        const bf16x8 b0 = tr_pair(L, vb0 + ks * 8192, vb0 + ks * 8192 + 1024);
        const bf16x8 b1 = tr_pair(L, vb1 + ks * 8192, vb1 + ks * 8192 + 1024);
        SCHED();
        o0 = MFMA32(pa[ks], b0, o0);
        o1 = MFMA32(pa[ks], b1, o1);
      }
      const u32x4 po = {__float_as_uint(o0[0]), tag, __float_as_uint(o1[0]), tag};
      __builtin_amdgcn_raw_buffer_store_b128(po, wsr, W_PO + (((4 * h + g) * 32 + i) * 128 + 32 * cw + 2 * c) * 8, 0, SC1);
      lds_flag(F_POF + 4 * cw, 1);
    }
    STAMP(9);
    {
      const int f = q4 | ((g & 1) << 2), fd = (4 * g + q4) & 7;
      bf16x8 wb[4];
      s16x4 db[4];
      wait_cnt(L, C_WO);
#pragma unroll
      for (int u = 0; u < 4; ++u) {
        const int a = L_WO + (8 * g + q4) * 512 + (((4 * cw + u) ^ f) * 32) + pp * 8;
        wb[u] = tr_pair(L, a, a + 2048);
      }
      wait_cnt(L, C_DN);
      STAMP(10);
#pragma unroll
      for (int u = 0; u < 4; ++u)
        db[u] = __builtin_amdgcn_ds_read_tr16_b64_v4i16((LAS s16x4*)(L + L_DN + (4 * g + q4) * 512 + (((4 * cw + u) ^ fd) * 32) + pp * 8));
      wait_flag1(L, F_HAF);
      const s16x4 ha = *(LAS s16x4*)(L + L_HA + g * 8);
      f32x4 dd[4], ss[4];
#pragma unroll
      for (int u = 0; u < 4; ++u) dd[u] = __builtin_amdgcn_mfma_f32_16x16x16bf16_1k(ha, db[u], f32x4{0.f, 0.f, 0.f, 0.f}, 0, 0, 0);
      const float dsel = sel4(g, dd[0][0], dd[1][0], dd[2][0], dd[3][0]);
      SCHED();
      wait_flag1(L, F_OAF);
      const bf16x8 oa = *(LAS bf16x8*)(L + L_OA + g * 16);
#pragma unroll
      for (int u = 0; u < 4; ++u) ss[u] = MFMA32(oa, wb[u], (f32x4{0.f, 0.f, 0.f, 0.f}));
      const float ssel = sel4(g, ss[0][0], ss[1][0], ss[2][0], ss[3][0]);
      const u32x4 o3 = {__float_as_uint(ssel), tag, __float_as_uint(dsel), tag};
      __builtin_amdgcn_raw_buffer_store_b128(o3, wsr, W_O3 + (rg * 512 + ch * 256 + cw * 64 + lane) * 16, 0, SC1);
      lds_flag(F_O3F + 4 * cw, 1);
    }
    STAMP(12);
    return;
  }

  if (wv == 6) {
    const int col0 = (h * 8 + (i >> 2)) * 8 + 2 * (i & 3);
    const unsigned xw = __builtin_amdgcn_readlane(xv[i & 3], col0 >> 3);
    const unsigned tag = __builtin_amdgcn_readfirstlane(e_in) * 1664525u + 1013904223u;
    const int r = rg >> 2, dc = rg & 3;
    wait_flag4(L, F_PF);
    wait_cnt(L, C_V0 + 4);
    STAMP(1);
    float w[8];
    {
      float m[1], l[1];
      gather<1, SLP>(wsr, W_ML + (r * 32 + (lane & 31)) * 16, 0, tag, m, l);
      const float M = wave_max(m[0]);
      const float e = __builtin_amdgcn_exp2f((m[0] - M) * LOG2E);
      const float Lsum = wave_sum(e * l[0]) * 0.5f;
      *(LAS float*)(L + L_WS + ((lane & 3) * 8 + ((lane & 31) >> 2)) * 4) = e * rcp(Lsum);
      const f32x4 w0 = *(LAS f32x4*)(L + L_WS + g * 32), w1 = *(LAS f32x4*)(L + L_WS + g * 32 + 16);
#pragma unroll
      for (int k = 0; k < 4; ++k) { w[k] = w0[k]; w[4 + k] = w1[k]; }
    }
    STAMP(2);
    wait_flag4(L, F_POF);
    STAMP(3);
    {
      float v0[8], v1[8];
      gather<8, 0>(wsr, W_PO + ((r * 32 + g) * 128 + 32 * dc + 2 * c) * 8, 4096, tag, v0, v1);
      STAMP(4);
      float a0 = 0.f, a1 = 0.f;
#pragma unroll
      for (int k = 0; k < 8; ++k) { a0 = fmaf(w[k], v0[k], a0); a1 = fmaf(w[k], v1[k], a1); }
      a0 = xrow_sum(a0); a1 = xrow_sum(a1);
      const unsigned ob = pack_bf(a0, a1);
      *(LAS u16*)(L + L_OA + c * 2) = (u16)ob;
      *(LAS u16*)(L + L_OA + 32 + c * 2) = (u16)(ob >> 16);
      LDS_WAIT();
      lds_flag(F_OAF, 1);
    }
    wait_flag4(L, F_O3F);
    STAMP(6);
    {
      float vs[4], vd[4];
      gather<4, 0>(wsr, W_O3 + ((lane >> 1) * 512 + col0 + (lane & 1)) * 16, 32 * 8192, tag, vs, vd);
      STAMP(7);
      float s = (vs[0] + vs[1]) + (vs[2] + vs[3]);
      float d = (vd[0] + vd[1]) + (vd[2] + vd[3]);
      s += dpp<XOR2>(s); d += dpp<XOR2>(d);
      s += dpp<ROR4>(s); d += dpp<ROR4>(d);
      s += dpp<ROR8>(s); d += dpp<ROR8>(d);
      s = xrow_sum(s); d = xrow_sum(d);
      const unsigned sd = pack_bf(s, d);
      s = bf_lo(sd); d = bf_hi(sd);
      const float gt = rbf(d * rcp(1.f + __builtin_amdgcn_exp2f(-d * LOG2E)));
      const float xe = (lane & 1) ? bf_hi(xw) : bf_lo(xw);
      const unsigned res = pack_bf(xe + rbf(s * gt), 0.f);
      if (lane < 2) *(GAS u16*)(out + col0 + lane) = (u16)res;
      if (b == 0 && lane == 0) __hip_atomic_store((GAS unsigned*)(ws + W_E), tag, __ATOMIC_RELAXED, __HIP_MEMORY_SCOPE_AGENT);
    }
    STAMP(8);
    {
      for (int z = 0; z < 8; ++z) __builtin_amdgcn_s_sleep(64);
      LDS_WAIT();
      unsigned t0 = *(volatile LAS unsigned*)(L + L_END + (DIAG_WV * 16 + DIAG_IDX0) * 4);
      unsigned t1 = *(volatile LAS unsigned*)(L + L_END + (DIAG_WV * 16 + DIAG_IDX) * 4);
      unsigned val = __builtin_amdgcn_readfirstlane(t1 - t0);
      if (val > 5000u) val = 5000u;
      const unsigned s0 = (unsigned)__builtin_amdgcn_s_memrealtime();
      while ((unsigned)__builtin_amdgcn_s_memrealtime() - s0 < val * DIAG_K) __builtin_amdgcn_s_sleep(8);
      if (lane == 0) *(GAS unsigned*)(ws + (12 << 20) + 4096 + b * 4) = (unsigned)__builtin_amdgcn_s_memrealtime();
    }
    return;
  }

  {
    const unsigned tag = __builtin_amdgcn_readfirstlane(e_in) * 1664525u + 1013904223u;
    wait_cnt(L, C_V0 + 8);
    STAMP(1);
    const GAS u64* src = (const GAS u64*)(ws + W_HG + (size_t)(g * DF + rg * 14 + min(c, 13)) * 8);
    u64 v;
#pragma nounroll
    for (unsigned n = 0; n < SPIN_GLB; ++n) {
      v = __hip_atomic_load(src, __ATOMIC_RELAXED, __HIP_MEMORY_SCOPE_AGENT);
      if (none((unsigned)(v >> 32) != tag)) break;
      __builtin_amdgcn_s_sleep(SLP);
    }
    STAMP(2);
    auto s = __builtin_amdgcn_permlane16_swap((unsigned)v, (unsigned)v, false, false);
    const float y = __uint_as_float(s[0]) + __uint_as_float(s[1]);
    auto t = __builtin_amdgcn_permlane32_swap(__float_as_uint(y), __float_as_uint(y), false, false);
    const unsigned gu = pack_bf(__uint_as_float(t[0]), __uint_as_float(t[1]));
    const float gg = bf_lo(gu), uu = bf_hi(gu);
    const float z = 0.7978845608028654f * (gg + 0.044715f * gg * gg * gg);
    const float ge = rbf(gg * rcp(1.f + __builtin_amdgcn_exp2f(-2.f * LOG2E * z)));
    const unsigned hb = c < 14 ? pack_bf(ge * uu, 0.f) : 0u;
    *(LAS u16*)(L + L_HA + c * 2) = (u16)hb;
    LDS_WAIT();
    lds_flag(F_HAF, 1);
    STAMP(3);
  }
  return;
  }

  {
    const int lw = wv;
    const int cnt_addr = F_CNT + 4 * lw;
    const size_t hoff = ((size_t)h * SK + (SK - WIN)) * 256;
    {
      const int row = lane >> 2, chunk = (lane & 3) ^ (((row >> 3) & 1) << 1);
      const char* src = (const char*)wq + (size_t)(q_rh * 256 + lw * 16) * 8192 + q_col0 * 2;
      const unsigned vo = row * 8192 + chunk * 16;
      issue_run<0, 8>([&](auto J) {
        constexpr int j = decltype(J)::value;
        DMA(src + (size_t)j * 32 * 8192 + vo, L + L_WQ + (2 * j + lw) * 1024);
        after_issue<j + 1>(cnt_addr);
      });
    }
    __builtin_amdgcn_s_barrier();
    {
      const char* src = (const char*)kc + hoff + lw * 1024;
      const unsigned vo0 = g * 256 + ((c ^ (4 * lw + g)) * 16), vo1 = g * 256 + ((c ^ (4 * (lw + 2) + g)) * 16) + 2048;
      issue_run<0, 16>([&](auto J) {
        constexpr int j = decltype(J)::value, blk = j >> 1;
        const char* s = src + (size_t)(blk * 32 + (i ^ ((blk & 1) << 1))) * 4096;
        DMA(s + ((j & 1) ? vo1 : vo0), L + L_K + (2 * j + lw) * 1024);
        after_issue<8 + j + 1>(cnt_addr);
      });
    }
    {
      const int row = lane >> 2, chunk = (lane & 3) ^ (((row >> 3) & 1) << 1);
      const char* src = (const char*)(u_mat ? wu : wg) + (size_t)(u_rh * 256 + lw * 16) * (DF * 2) + u_col0 * 2;
      const unsigned vo = row * (DF * 2) + chunk * 16;
      issue_run<0, 8>([&](auto J) {
        constexpr int j = decltype(J)::value;
        DMA(src + (size_t)j * 32 * (DF * 2) + vo, L + L_GU + (2 * j + lw) * 1024);
        after_issue<24 + j + 1>(cnt_addr);
      });
    }
    {
      const char* src = (const char*)vc + hoff + lw * 1024;
      const unsigned vo0 = g * 256 + ((c ^ (g << 1)) * 16), vo1 = g * 256 + ((c ^ ((g | 4) << 1)) * 16) + 2048;
      issue_run<0, 16>([&](auto J) {
        constexpr int j = decltype(J)::value, blk = j >> 1;
        const char* s = src + (size_t)(blk * 32 + (i ^ ((blk & 1) << 1))) * 4096;
        DMA(s + ((j & 1) ? vo1 : vo0), L + L_V + (2 * j + lw) * 1024);
        after_issue<32 + j + 1>(cnt_addr);
      });
    }
    {
      const int r2 = lane >> 5, pos = lane & 31;
      const char* src = (const char*)wo + (size_t)(rg * 32 + 2 * lw) * 1024 + ch * 512;
      const unsigned vo0 = r2 * 1024 + ((pos ^ ((2 * lw + r2) << 1)) * 16), vo1 = r2 * 1024 + ((pos ^ (((2 * lw + r2) | 4) << 1)) * 16);
      issue_run<0, 8>([&](auto J) {
        constexpr int j = decltype(J)::value;
        DMA(src + j * 4096 + (((j >> 1) & 1) ? vo1 : vo0), L + L_WO + (2 * j + lw) * 1024);
        after_issue<48 + j + 1>(cnt_addr);
      });
      const char* srd = (const char*)wd + (size_t)(rg * 14 + 2 * lw) * 1024 + ch * 512;
      issue_run<0, 4>([&](auto J) {
        constexpr int j = decltype(J)::value;
        const int back = (j == 3) ? lw * 2048 : 0;
        DMA(srd + j * 4096 - back + ((j & 1) ? vo1 : vo0), L + L_DN + (2 * j + lw) * 1024);
        after_issue<56 + j + 1>(cnt_addr);
      });
    }
    drain_to<C_WQ>(cnt_addr); drain_to<C_K>(cnt_addr); drain_to<C_GU>(cnt_addr);
    drain_to<36>(cnt_addr); drain_to<40>(cnt_addr); drain_to<44>(cnt_addr); drain_to<48>(cnt_addr);
    drain_to<C_WO>(cnt_addr); drain_to<C_DN>(cnt_addr);
    STAMP(2);
  }
}

__global__ void k_nop() {}
constexpr int W_T = 12 << 20;
__global__ void k_pre(char* ws) {
  if (threadIdx.x == 0) *(GAS unsigned*)(ws + W_T + blockIdx.x * 4) = (unsigned)__builtin_amdgcn_s_memrealtime();
}
__global__ void k_post(char* ws) {
  const unsigned t0 = (unsigned)__builtin_amdgcn_s_memrealtime();
  unsigned best = 0xffffffffu;
  for (int k = threadIdx.x; k < 256; k += 64) { unsigned d = t0 - *(volatile GAS unsigned*)(ws + W_T + 4096 + k * 4); best = d < best ? d : best; }
  for (int o = 32; o; o >>= 1) { unsigned d = __shfl_xor(best, o); best = d < best ? d : best; }
  unsigned val = __builtin_amdgcn_readfirstlane(best);
  if (val > 5000u) val = 5000u;
  const unsigned s0 = (unsigned)__builtin_amdgcn_s_memrealtime();
  while ((unsigned)__builtin_amdgcn_s_memrealtime() - s0 < val * POST_K) __builtin_amdgcn_s_sleep(8);
}
extern "C" void kernel_launch(void* const* d_in, const int* in_sizes, int n_in, void* d_out, int out_size, void* d_ws,
                              size_t ws_size, hipStream_t stream) {
  (void)in_sizes; (void)n_in; (void)out_size; (void)ws_size;
  (void)hipFuncSetAttribute((const void*)fused_decode, hipFuncAttributeMaxDynamicSharedMemorySize, L_END + 512);
  fused_decode<<<256, 512, L_END + 512, stream>>>((const u16*)d_in[0], (const u16*)d_in[1], (const u16*)d_in[2], (const u16*)d_in[3],
                                            (const u16*)d_in[4], (const u16*)d_in[6], (const u16*)d_in[7], (const u16*)d_in[8],
                                            (u16*)d_out, (char*)d_ws + WS_OFF(ws_size));
  k_nop<<<POST_EMPTY, 64, 0, stream>>>();
}
```

```cpp
#define DIAG_WV 6
#define DIAG_IDX 0
#define DIAG_IDX0 0
#define DIAG_K 0
#define DIAG_CLK 0
#define PRE_T 1
#define POST_K 0
#define PRE_SPIN 1000
#include <hip/hip_runtime.h>
#include <hip/hip_bf16.h>
#include <stdint.h>
#include <type_traits>

typedef unsigned short u16;
typedef unsigned long long u64;
typedef unsigned u32x4 __attribute__((ext_vector_type(4)));
typedef unsigned u32x2 __attribute__((ext_vector_type(2)));
typedef float f32x4 __attribute__((ext_vector_type(4)));
typedef float f32x2 __attribute__((ext_vector_type(2)));
typedef short s16x4 __attribute__((ext_vector_type(4)));
typedef short s16x8 __attribute__((ext_vector_type(8)));
typedef __bf16 bf16x8 __attribute__((ext_vector_type(8)));
typedef __bf16 bf16x2 __attribute__((ext_vector_type(2)));
#define LAS __attribute__((address_space(3)))
#define GAS __attribute__((address_space(1)))

#define THR 10
#define STAMPS 0
#define CODE_KB 0
#define LSLP 0
#define DBG 0
#define WS_OFF(s) 0
#define SLP 2

constexpr int SK = 65536, WIN = 4096, DF = 1792;
constexpr int L_WQ = 0, L_K = 16384, L_GU = 49152, L_V = 65536, L_WO = 98304, L_DN = 114688, L_MISC = 122880;
constexpr int L_XN = L_MISC, L_QA = L_XN + 1024, L_PB = L_QA + 1024, L_WS = L_PB + 1280, L_OA = L_WS + 128, L_HA = L_OA + 64,
              L_FL = L_HA + 64, L_END = L_FL + 128;
constexpr int F_CNT = L_FL, F_PF = L_FL + 16, F_POF = L_FL + 32, F_O3F = L_FL + 48, F_OAF = L_FL + 64, F_HAF = L_FL + 68;
constexpr int W_E = 0, W_QG = 4096, W_HG = W_QG + 65536, W_ML = W_HG + 65536, W_PO = W_ML + 16384 + 4096 * 3,
              W_O3 = W_PO + (1 << 20), W_END = W_O3 + (1 << 20);
constexpr int W_ST = 8 << 20;
constexpr int C_WQ = 8, C_K = 24, C_GU = 32, C_V0 = 36, C_WO = 56, C_DN = 60, C_ALL = 60;
constexpr unsigned SPIN_LDS = 1u << 22, SPIN_GLB = 1u << 17;
constexpr float LOG2E = 1.4426950408889634f;
constexpr int OOB = 0x7ffffff0;

__device__ __forceinline__ float bf_lo(unsigned w) { return __uint_as_float(w << 16); }
__device__ __forceinline__ float bf_hi(unsigned w) { return __uint_as_float(w & 0xffff0000u); }
__device__ __forceinline__ unsigned pack_bf(float lo, float hi) {
  f32x2 v = {lo, hi};
  return __builtin_bit_cast(unsigned, __builtin_convertvector(v, bf16x2));
}
__device__ __forceinline__ float rbf(float f) { return bf_lo(pack_bf(f, f)); }
__device__ __forceinline__ float rcp(float f) { return __builtin_amdgcn_rcpf(f); }
__device__ __forceinline__ float vmax(float a, float b) { float r; asm("v_max_f32 %0, %1, %2" : "=v"(r) : "v"(a), "v"(b)); return r; }
template <int CTRL> __device__ __forceinline__ float dpp(float x) {
  return __builtin_bit_cast(float, __builtin_amdgcn_mov_dpp(__builtin_bit_cast(int, x), CTRL, 0xf, 0xf, true));
}
constexpr int XOR1 = 0xB1, XOR2 = 0x4E, ROR4 = 0x124, ROR8 = 0x128;
__device__ __forceinline__ float xrow_sum(float x) {
  auto s = __builtin_amdgcn_permlane16_swap(__float_as_uint(x), __float_as_uint(x), false, false);
  x = __uint_as_float(s[0]) + __uint_as_float(s[1]);
  auto t = __builtin_amdgcn_permlane32_swap(__float_as_uint(x), __float_as_uint(x), false, false);
  return __uint_as_float(t[0]) + __uint_as_float(t[1]);
}
__device__ __forceinline__ float xrow_max(float x) {
  auto s = __builtin_amdgcn_permlane16_swap(__float_as_uint(x), __float_as_uint(x), false, false);
  x = fmaxf(__uint_as_float(s[0]), __uint_as_float(s[1]));
  auto t = __builtin_amdgcn_permlane32_swap(__float_as_uint(x), __float_as_uint(x), false, false);
  return fmaxf(__uint_as_float(t[0]), __uint_as_float(t[1]));
}
__device__ __forceinline__ float wave_sum(float x) {
  x += dpp<XOR1>(x); x += dpp<XOR2>(x); x += dpp<ROR4>(x); x += dpp<ROR8>(x);
  return xrow_sum(x);
}
__device__ __forceinline__ float wave_max(float x) {
  x = fmaxf(x, dpp<XOR1>(x)); x = fmaxf(x, dpp<XOR2>(x)); x = fmaxf(x, dpp<ROR4>(x)); x = fmaxf(x, dpp<ROR8>(x));
  return xrow_max(x);
}
__device__ __forceinline__ float sel4(int g, float a0, float a1, float a2, float a3) {
  const float lo = (g & 1) ? a1 : a0, hi = (g & 1) ? a3 : a2;
  return (g & 2) ? hi : lo;
}
__device__ __forceinline__ bool none(bool bad) { return __builtin_amdgcn_ballot_w64(bad) == 0ull; }
#define SCHED() __builtin_amdgcn_sched_barrier(0)
#define MEMBAR() asm volatile("" ::: "memory")
#define LDS_WAIT() asm volatile("s_waitcnt lgkmcnt(0)" ::: "memory")
__device__ __forceinline__ void lds_flag(int addr, unsigned v) {
  asm volatile("ds_write_b32 %0, %1" ::"v"(addr), "v"(v) : "memory");
}
__device__ __forceinline__ void wait_cnt(LAS char* L, unsigned thr) {
#pragma nounroll
  for (unsigned n = 0; n < SPIN_LDS; ++n) {
    u32x2 c = *(volatile LAS u32x2*)(L + F_CNT);
    if (__builtin_amdgcn_readfirstlane(min(c[0], c[1])) >= thr) break;
    if (LSLP) __builtin_amdgcn_s_sleep(LSLP);
  }
  MEMBAR();
}
__device__ __forceinline__ void wait_flag4(LAS char* L, int off) {
#pragma nounroll
  for (unsigned n = 0; n < SPIN_LDS; ++n) {
    u32x4 c = *(volatile LAS u32x4*)(L + off);
    if (__builtin_amdgcn_readfirstlane(c[0] & c[1] & c[2] & c[3])) break;
    if (LSLP) __builtin_amdgcn_s_sleep(LSLP);
  }
  MEMBAR();
}
__device__ __forceinline__ void wait_flag1(LAS char* L, int off) {
#pragma nounroll
  for (unsigned n = 0; n < SPIN_LDS; ++n) {
    unsigned c = *(volatile LAS unsigned*)(L + off);
    if (__builtin_amdgcn_readfirstlane(c)) break;
    if (LSLP) __builtin_amdgcn_s_sleep(LSLP);
  }
  MEMBAR();
}
__device__ __forceinline__ bf16x8 tr_pair(LAS char* L, int a0, int a1) {
  s16x4 t0 = __builtin_amdgcn_ds_read_tr16_b64_v4i16((LAS s16x4*)(L + a0));
  s16x4 t1 = __builtin_amdgcn_ds_read_tr16_b64_v4i16((LAS s16x4*)(L + a1));
  s16x8 b = {t0[0], t0[1], t0[2], t0[3], t1[0], t1[1], t1[2], t1[3]};
  return __builtin_bit_cast(bf16x8, b);
}
#define MFMA32(a, b, c) __builtin_amdgcn_mfma_f32_16x16x32_bf16((a), (b), (c), 0, 0, 0)
#define DMA(src, dst) __builtin_amdgcn_global_load_lds((const unsigned*)(src), (LAS unsigned*)(dst), 16, 0, 2)
#define SC1 16
#define LD4(off, soff) __builtin_bit_cast(u32x4, __builtin_amdgcn_raw_buffer_load_b128(wsr, (off), (soff), SC1))

template <int N, int SLEEP>
__device__ __forceinline__ void gather(__amdgpu_buffer_rsrc_t wsr, int off, int stride, unsigned tag, float (&lo)[N], float (&hi)[N]) {
  int o[N];
#pragma unroll
  for (int k = 0; k < N; ++k) { o[k] = off; lo[k] = 0.f; hi[k] = 0.f; }
#pragma nounroll
  for (unsigned n = 0; n < SPIN_GLB; ++n) {
    MEMBAR();
    u32x4 v[N];
#pragma unroll
    for (int k = 0; k < N; ++k) v[k] = LD4(o[k], k * stride);
    bool pend = false;
#pragma unroll
    for (int k = 0; k < N; ++k) {
      const bool ok = (v[k][1] == tag) & (v[k][3] == tag) & (o[k] != OOB);
      lo[k] = ok ? __uint_as_float(v[k][0]) : lo[k];
      hi[k] = ok ? __uint_as_float(v[k][2]) : hi[k];
      o[k] = ok ? OOB : o[k];
      pend |= o[k] != OOB;
    }
    if (none(pend)) break;
    if (SLEEP) __builtin_amdgcn_s_sleep(SLEEP);
  }
}

#define STAMP(k) do { unsigned t_ = (unsigned)__builtin_amdgcn_s_memrealtime(); lds_flag(L_END + (wv * 16 + (k)) * 4, t_); } while (0)

template <int J> __device__ __forceinline__ void after_issue(int cnt_addr) {
  if constexpr (J > THR) {
    asm volatile("s_waitcnt vmcnt(%0)" ::"n"(THR) : "memory");
    constexpr int LANDED = J - THR;
    if constexpr (LANDED == C_WQ || LANDED == C_K || LANDED == C_GU || (LANDED >= C_V0 && LANDED <= 48 && (LANDED & 3) == 0) ||
                  LANDED == C_WO)
      lds_flag(cnt_addr, LANDED);
  }
}
template <int LANDED> __device__ __forceinline__ void drain_to(int cnt_addr) {
  if constexpr (LANDED > C_ALL - THR) {
    asm volatile("s_waitcnt vmcnt(%0)" ::"n"(C_ALL - LANDED) : "memory");
    lds_flag(cnt_addr, LANDED);
  }
}
template <int J0, int N, typename F> __device__ __forceinline__ void issue_run(F f) {
  if constexpr (N > 0) {
    f(std::integral_constant<int, J0>{});
    issue_run<J0 + 1, N - 1>(f);
  }
}

__global__ void __launch_bounds__(512, 2) __attribute__((amdgpu_waves_per_eu(2, 2)))
fused_decode(const u16* __restrict__ x, const u16* __restrict__ kc, const u16* __restrict__ vc, const u16* __restrict__ wq,
             const u16* __restrict__ wo, const u16* __restrict__ wg, const u16* __restrict__ wu, const u16* __restrict__ wd,
             u16* __restrict__ out, char* ws) {
  extern __shared__ __attribute__((aligned(16))) char lds_raw[];
  LAS char* L = (LAS char*)lds_raw;
  const int b = blockIdx.x, tid = threadIdx.x, lane = tid & 63;
  const int wv = __builtin_amdgcn_readfirstlane(tid >> 6);
  const int h = b & 7, i = b >> 3;
  const int g = lane >> 4, c = lane & 15;
  const int q_rh = i >> 4, q_col0 = 512 * h + 32 * (i & 15);
  const int gi = i < 28 ? i : i - 4;
  const int combo = (gi >> 1) * 8 + h;
  const int u_mat = (combo >> 1) & 1, u_rh = combo & 1, u_col0 = (combo >> 2) * 64 + (gi & 1) * 32;
  const int rg = h * 16 + (i >> 1), ch = i & 1;
  STAMP(0);

  if (wv >= 2) {
  if (wv == 7 && lane < 32) *(LAS unsigned*)(L + L_FL + lane * 4) = 0u;
  __builtin_amdgcn_s_setprio(2);
  const __amdgpu_buffer_rsrc_t wsr = __builtin_amdgcn_make_buffer_rsrc((void*)ws, 0, W_END, 0x00020000);
  const u32x4 xv = *(const GAS u32x4*)(x + lane * 8);
  const unsigned e_in = __hip_atomic_load((GAS unsigned*)(ws + W_E), __ATOMIC_RELAXED, __HIP_MEMORY_SCOPE_AGENT);
  SCHED();
  LDS_WAIT();
  __builtin_amdgcn_s_barrier();
  SCHED();

  if (wv < 6) {
    const int cw = wv - 2;
    {
      float f[8];
#pragma unroll
      for (int k = 0; k < 4; ++k) { f[2 * k] = bf_lo(xv[k]); f[2 * k + 1] = bf_hi(xv[k]); }
      float s = ((f[0] + f[1]) + (f[2] + f[3])) + ((f[4] + f[5]) + (f[6] + f[7]));
      const float mean = wave_sum(s) * (1.f / 512.f);
      float v0 = 0.f, v1 = 0.f;
#pragma unroll
      for (int k = 0; k < 8; k += 2) { const float d0 = f[k] - mean, d1 = f[k + 1] - mean; v0 = fmaf(d0, d0, v0); v1 = fmaf(d1, d1, v1); }
      const float var = wave_sum(v0 + v1) * (1.f / 512.f);
      const float mb = rbf(mean);
      const float den = rbf(__builtin_amdgcn_sqrtf(rbf(rbf(var) + 1e-5f)));
      const float inv = rcp(den);
      u32x4 xn;
#pragma unroll
      for (int k = 0; k < 4; ++k) {
        const unsigned d = pack_bf(f[2 * k] - mb, f[2 * k + 1] - mb);
        float a0 = bf_lo(d), a1 = bf_hi(d);
        float q0 = a0 * inv, q1 = a1 * inv;
        q0 = fmaf(fmaf(-q0, den, a0), inv, q0); q1 = fmaf(fmaf(-q1, den, a1), inv, q1);
        xn[k] = pack_bf(q0, q1);
      }
      *(LAS u32x4*)(L + L_XN + lane * 16) = xn;
    }
    const unsigned tag = __builtin_amdgcn_readfirstlane(e_in) * 1664525u + 1013904223u;
    STAMP(1);
    const int q4 = (lane & 15) >> 2, pp = lane & 3;
    const int p_tile = cw < 2 ? L_WQ : L_GU, p_rh = cw < 2 ? q_rh : u_rh;
    bf16x8 xa[8];
#pragma unroll
    for (int ks = 0; ks < 8; ++ks) xa[ks] = *(LAS bf16x8*)(L + L_XN + p_rh * 512 + g * 16 + ks * 64);
    auto proj = [&]() -> float {
      const int tb = p_tile + (8 * g + q4) * 64 + (((2 * (cw & 1) + (pp >> 1)) ^ ((g & 1) << 1)) * 16) + 8 * (pp & 1);
      bf16x8 wb[8];
#pragma unroll
      for (int ks = 0; ks < 8; ++ks) wb[ks] = tr_pair(L, tb + ks * 2048, tb + ks * 2048 + 256);
      SCHED();
      f32x4 a0 = {0.f, 0.f, 0.f, 0.f}, a1 = {0.f, 0.f, 0.f, 0.f};
#pragma unroll
      for (int ks = 0; ks < 8; ks += 2) { a0 = MFMA32(xa[ks], wb[ks], a0); a1 = MFMA32(xa[ks + 1], wb[ks + 1], a1); }
      return a0[0] + a1[0];
    };
    if (cw < 2) {
      wait_cnt(L, C_WQ);
      STAMP(2);
      const float qp = proj();
      if (lane < 16)
        __hip_atomic_store((GAS u64*)(ws + W_QG + (size_t)(q_rh * 4096 + q_col0 + 16 * cw + lane) * 8),
                           ((u64)tag << 32) | __float_as_uint(qp), __ATOMIC_RELAXED, __HIP_MEMORY_SCOPE_AGENT);
      STAMP(3);
    }
    wait_cnt(L, C_K);
    STAMP(4);
    {
      bf16x8 kb[8][4];
#pragma unroll
      for (int kt = 0; kt < 8; ++kt)
#pragma unroll
        for (int ks = 0; ks < 4; ++ks) kb[kt][ks] = *(LAS bf16x8*)(L + L_K + kt * 4096 + c * 256 + (((4 * ks + g) ^ c) * 16));
      SCHED();
      float qlo[2], qhi[2];
      gather<2, SLP>(wsr, W_QG + ((4 * h + cw) * 128 + 2 * lane) * 8, 32768, tag, qlo, qhi);
      STAMP(5);
      *(LAS unsigned*)(L + L_QA + cw * 256 + lane * 4) = pack_bf(qlo[0] + qlo[1], qhi[0] + qhi[1]);
      bf16x8 qa[4];
#pragma unroll
      for (int ks = 0; ks < 4; ++ks) qa[ks] = *(LAS bf16x8*)(L + L_QA + cw * 256 + ks * 64 + g * 16);
      SCHED();
      f32x4 sc[8];
#pragma unroll
      for (int kt = 0; kt < 8; ++kt) sc[kt] = MFMA32(qa[0], kb[kt][0], (f32x4{0.f, 0.f, 0.f, 0.f}));
#pragma unroll
      for (int ks = 1; ks < 4; ++ks)
#pragma unroll
        for (int kt = 0; kt < 8; ++kt) sc[kt] = MFMA32(qa[ks], kb[kt][ks], sc[kt]);
      float s0 = sel4(g, sc[0][0], sc[2][0], sc[4][0], sc[6][0]);
      float s1 = sel4(g, sc[1][0], sc[3][0], sc[5][0], sc[7][0]);
      unsigned r = pack_bf(s0, s1);
      r = pack_bf(bf_lo(r) * 0.08838834764831845f, bf_hi(r) * 0.08838834764831845f);
      s0 = bf_lo(r); s1 = bf_hi(r);
      const float m = wave_max(fmaxf(s0, s1));
      const float p0 = __builtin_amdgcn_exp2f((s0 - m) * LOG2E), p1 = __builtin_amdgcn_exp2f((s1 - m) * LOG2E);
      const unsigned pb = pack_bf(p0, p1);
      *(LAS u16*)(L + L_PB + cw * 320 + (32 * g + c) * 2) = (u16)pb;
      *(LAS u16*)(L + L_PB + cw * 320 + (32 * g + 16 + c) * 2) = (u16)(pb >> 16);
      const float l = wave_sum(p0 + p1);
      LDS_WAIT();
      lds_flag(F_PF + 4 * cw, 1);
      if (lane == 0) {
        const u32x4 ml = {__float_as_uint(m), tag, __float_as_uint(l), tag};
        __builtin_amdgcn_raw_buffer_store_b128(ml, wsr, W_ML + ((4 * h + cw) * 32 + i) * 16, 0, SC1);
      }
    }
    STAMP(6);
    if (cw >= 2) {
      wait_cnt(L, C_GU);
      const float up = proj();
      if (lane < 16 && i < 28)
        __hip_atomic_store((GAS u64*)(ws + W_HG + (size_t)((u_mat * 2 + u_rh) * DF + u_col0 + 16 * (cw - 2) + lane) * 8),
                           ((u64)tag << 32) | __float_as_uint(up), __ATOMIC_RELAXED, __HIP_MEMORY_SCOPE_AGENT);
      STAMP(7);
    }
    {
      wait_flag4(L, F_PF);
      bf16x8 pa[4];
#pragma unroll
      for (int ks = 0; ks < 4; ++ks) pa[ks] = *(LAS bf16x8*)(L + L_PB + (c >> 2) * 320 + ks * 64 + g * 16);
      const int f = q4 | ((g & 1) << 2);
      const int vb0 = L_V + (8 * g + q4) * 256 + (((2 * cw) ^ f) * 32) + pp * 8;
      const int vb1 = L_V + (8 * g + q4) * 256 + (((2 * cw + 1) ^ f) * 32) + pp * 8;
      f32x4 o0 = {0.f, 0.f, 0.f, 0.f}, o1 = {0.f, 0.f, 0.f, 0.f};
#pragma unroll
      for (int ks = 0; ks < 4; ++ks) {
        wait_cnt(L, C_V0 + 4 * ks);
        const bf16x8 b0 = tr_pair(L, vb0 + ks * 8192, vb0 + ks * 8192 + 1024);
        const bf16x8 b1 = tr_pair(L, vb1 + ks * 8192, vb1 + ks * 8192 + 1024);
        SCHED();
        o0 = MFMA32(pa[ks], b0, o0);
        o1 = MFMA32(pa[ks], b1, o1);
      }
      const u32x4 po = {__float_as_uint(o0[0]), tag, __float_as_uint(o1[0]), tag};
      __builtin_amdgcn_raw_buffer_store_b128(po, wsr, W_PO + (((4 * h + g) * 32 + i) * 128 + 32 * cw + 2 * c) * 8, 0, SC1);
      lds_flag(F_POF + 4 * cw, 1);
    }
    STAMP(9);
    {
      const int f = q4 | ((g & 1) << 2), fd = (4 * g + q4) & 7;
      bf16x8 wb[4];
      s16x4 db[4];
      wait_cnt(L, C_WO);
#pragma unroll
      for (int u = 0; u < 4; ++u) {
        const int a = L_WO + (8 * g + q4) * 512 + (((4 * cw + u) ^ f) * 32) + pp * 8;
        wb[u] = tr_pair(L, a, a + 2048);
      }
      wait_cnt(L, C_DN);
      STAMP(10);
#pragma unroll
      for (int u = 0; u < 4; ++u)
        db[u] = __builtin_amdgcn_ds_read_tr16_b64_v4i16((LAS s16x4*)(L + L_DN + (4 * g + q4) * 512 + (((4 * cw + u) ^ fd) * 32) + pp * 8));
      wait_flag1(L, F_HAF);
      const s16x4 ha = *(LAS s16x4*)(L + L_HA + g * 8);
      f32x4 dd[4], ss[4];
#pragma unroll
      for (int u = 0; u < 4; ++u) dd[u] = __builtin_amdgcn_mfma_f32_16x16x16bf16_1k(ha, db[u], f32x4{0.f, 0.f, 0.f, 0.f}, 0, 0, 0);
      const float dsel = sel4(g, dd[0][0], dd[1][0], dd[2][0], dd[3][0]);
      SCHED();
      wait_flag1(L, F_OAF);
      const bf16x8 oa = *(LAS bf16x8*)(L + L_OA + g * 16);
#pragma unroll
      for (int u = 0; u < 4; ++u) ss[u] = MFMA32(oa, wb[u], (f32x4{0.f, 0.f, 0.f, 0.f}));
      const float ssel = sel4(g, ss[0][0], ss[1][0], ss[2][0], ss[3][0]);
      const u32x4 o3 = {__float_as_uint(ssel), tag, __float_as_uint(dsel), tag};
      __builtin_amdgcn_raw_buffer_store_b128(o3, wsr, W_O3 + (rg * 512 + ch * 256 + cw * 64 + lane) * 16, 0, SC1);
      lds_flag(F_O3F + 4 * cw, 1);
    }
    STAMP(12);
    return;
  }

  if (wv == 6) {
    const int col0 = (h * 8 + (i >> 2)) * 8 + 2 * (i & 3);
    const unsigned xw = __builtin_amdgcn_readlane(xv[i & 3], col0 >> 3);
    const unsigned tag = __builtin_amdgcn_readfirstlane(e_in) * 1664525u + 1013904223u;
    const int r = rg >> 2, dc = rg & 3;
    wait_flag4(L, F_PF);
    wait_cnt(L, C_V0 + 4);
    STAMP(1);
    float w[8];
    {
      float m[1], l[1];
      gather<1, SLP>(wsr, W_ML + (r * 32 + (lane & 31)) * 16, 0, tag, m, l);
      const float M = wave_max(m[0]);
      const float e = __builtin_amdgcn_exp2f((m[0] - M) * LOG2E);
      const float Lsum = wave_sum(e * l[0]) * 0.5f;
      *(LAS float*)(L + L_WS + ((lane & 3) * 8 + ((lane & 31) >> 2)) * 4) = e * rcp(Lsum);
      const f32x4 w0 = *(LAS f32x4*)(L + L_WS + g * 32), w1 = *(LAS f32x4*)(L + L_WS + g * 32 + 16);
#pragma unroll
      for (int k = 0; k < 4; ++k) { w[k] = w0[k]; w[4 + k] = w1[k]; }
    }
    STAMP(2);
    wait_flag4(L, F_POF);
    STAMP(3);
    {
      float v0[8], v1[8];
      gather<8, 0>(wsr, W_PO + ((r * 32 + g) * 128 + 32 * dc + 2 * c) * 8, 4096, tag, v0, v1);
      STAMP(4);
      float a0 = 0.f, a1 = 0.f;
#pragma unroll
      for (int k = 0; k < 8; ++k) { a0 = fmaf(w[k], v0[k], a0); a1 = fmaf(w[k], v1[k], a1); }
      a0 = xrow_sum(a0); a1 = xrow_sum(a1);
      const unsigned ob = pack_bf(a0, a1);
      *(LAS u16*)(L + L_OA + c * 2) = (u16)ob;
      *(LAS u16*)(L + L_OA + 32 + c * 2) = (u16)(ob >> 16);
      LDS_WAIT();
      lds_flag(F_OAF, 1);
    }
    wait_flag4(L, F_O3F);
    STAMP(6);
    {
      float vs[4], vd[4];
      gather<4, 0>(wsr, W_O3 + ((lane >> 1) * 512 + col0 + (lane & 1)) * 16, 32 * 8192, tag, vs, vd);
      STAMP(7);
      float s = (vs[0] + vs[1]) + (vs[2] + vs[3]);
      float d = (vd[0] + vd[1]) + (vd[2] + vd[3]);
      s += dpp<XOR2>(s); d += dpp<XOR2>(d);
      s += dpp<ROR4>(s); d += dpp<ROR4>(d);
      s += dpp<ROR8>(s); d += dpp<ROR8>(d);
      s = xrow_sum(s); d = xrow_sum(d);
      const unsigned sd = pack_bf(s, d);
      s = bf_lo(sd); d = bf_hi(sd);
      const float gt = rbf(d * rcp(1.f + __builtin_amdgcn_exp2f(-d * LOG2E)));
      const float xe = (lane & 1) ? bf_hi(xw) : bf_lo(xw);
      const unsigned res = pack_bf(xe + rbf(s * gt), 0.f);
      if (lane < 2) *(GAS u16*)(out + col0 + lane) = (u16)res;
      if (b == 0 && lane == 0) __hip_atomic_store((GAS unsigned*)(ws + W_E), tag, __ATOMIC_RELAXED, __HIP_MEMORY_SCOPE_AGENT);
    }
    STAMP(8);
    {
      for (int z = 0; z < 8; ++z) __builtin_amdgcn_s_sleep(64);
      LDS_WAIT();
      unsigned t0 = *(volatile LAS unsigned*)(L + L_END + (DIAG_WV * 16 + DIAG_IDX0) * 4);
      unsigned t1 = *(volatile LAS unsigned*)(L + L_END + (DIAG_WV * 16 + DIAG_IDX) * 4);
      unsigned val = __builtin_amdgcn_readfirstlane(t1 - t0);
      {
        unsigned best = 0xffffffffu;
        for (int k = lane; k < 256; k += 64) { unsigned d = t0 - *(volatile GAS unsigned*)(ws + (12 << 20) + k * 4); best = d < best ? d : best; }
        for (int o = 32; o; o >>= 1) { unsigned d = __shfl_xor(best, o); best = d < best ? d : best; }
        val = __builtin_amdgcn_readfirstlane(best);
      }
      if (val > 5000u) val = 5000u;
      const unsigned s0 = (unsigned)__builtin_amdgcn_s_memrealtime();
      while ((unsigned)__builtin_amdgcn_s_memrealtime() - s0 < val * DIAG_K) __builtin_amdgcn_s_sleep(8);
      if (lane == 0) *(GAS unsigned*)(ws + (12 << 20) + 4096 + b * 4) = (unsigned)__builtin_amdgcn_s_memrealtime();
    }
    return;
  }

  {
    const unsigned tag = __builtin_amdgcn_readfirstlane(e_in) * 1664525u + 1013904223u;
    wait_cnt(L, C_V0 + 8);
    STAMP(1);
    const GAS u64* src = (const GAS u64*)(ws + W_HG + (size_t)(g * DF + rg * 14 + min(c, 13)) * 8);
    u64 v;
#pragma nounroll
    for (unsigned n = 0; n < SPIN_GLB; ++n) {
      v = __hip_atomic_load(src, __ATOMIC_RELAXED, __HIP_MEMORY_SCOPE_AGENT);
      if (none((unsigned)(v >> 32) != tag)) break;
      __builtin_amdgcn_s_sleep(SLP);
    }
    STAMP(2);
    auto s = __builtin_amdgcn_permlane16_swap((unsigned)v, (unsigned)v, false, false);
    const float y = __uint_as_float(s[0]) + __uint_as_float(s[1]);
    auto t = __builtin_amdgcn_permlane32_swap(__float_as_uint(y), __float_as_uint(y), false, false);
    const unsigned gu = pack_bf(__uint_as_float(t[0]), __uint_as_float(t[1]));
    const float gg = bf_lo(gu), uu = bf_hi(gu);
    const float z = 0.7978845608028654f * (gg + 0.044715f * gg * gg * gg);
    const float ge = rbf(gg * rcp(1.f + __builtin_amdgcn_exp2f(-2.f * LOG2E * z)));
    const unsigned hb = c < 14 ? pack_bf(ge * uu, 0.f) : 0u;
    *(LAS u16*)(L + L_HA + c * 2) = (u16)hb;
    LDS_WAIT();
    lds_flag(F_HAF, 1);
    STAMP(3);
  }
  return;
  }

  {
    const int lw = wv;
    const int cnt_addr = F_CNT + 4 * lw;
    const size_t hoff = ((size_t)h * SK + (SK - WIN)) * 256;
    {
      const int row = lane >> 2, chunk = (lane & 3) ^ (((row >> 3) & 1) << 1);
      const char* src = (const char*)wq + (size_t)(q_rh * 256 + lw * 16) * 8192 + q_col0 * 2;
      const unsigned vo = row * 8192 + chunk * 16;
      issue_run<0, 8>([&](auto J) {
        constexpr int j = decltype(J)::value;
        DMA(src + (size_t)j * 32 * 8192 + vo, L + L_WQ + (2 * j + lw) * 1024);
        after_issue<j + 1>(cnt_addr);
      });
    }
    __builtin_amdgcn_s_barrier();
    {
      const char* src = (const char*)kc + hoff + lw * 1024;
      const unsigned vo0 = g * 256 + ((c ^ (4 * lw + g)) * 16), vo1 = g * 256 + ((c ^ (4 * (lw + 2) + g)) * 16) + 2048;
      issue_run<0, 16>([&](auto J) {
        constexpr int j = decltype(J)::value, blk = j >> 1;
        const char* s = src + (size_t)(blk * 32 + (i ^ ((blk & 1) << 1))) * 4096;
        DMA(s + ((j & 1) ? vo1 : vo0), L + L_K + (2 * j + lw) * 1024);
        after_issue<8 + j + 1>(cnt_addr);
      });
    }
    {
      const int row = lane >> 2, chunk = (lane & 3) ^ (((row >> 3) & 1) << 1);
      const char* src = (const char*)(u_mat ? wu : wg) + (size_t)(u_rh * 256 + lw * 16) * (DF * 2) + u_col0 * 2;
      const unsigned vo = row * (DF * 2) + chunk * 16;
      issue_run<0, 8>([&](auto J) {
        constexpr int j = decltype(J)::value;
        DMA(src + (size_t)j * 32 * (DF * 2) + vo, L + L_GU + (2 * j + lw) * 1024);
        after_issue<24 + j + 1>(cnt_addr);
      });
    }
    {
      const char* src = (const char*)vc + hoff + lw * 1024;
      const unsigned vo0 = g * 256 + ((c ^ (g << 1)) * 16), vo1 = g * 256 + ((c ^ ((g | 4) << 1)) * 16) + 2048;
      issue_run<0, 16>([&](auto J) {
        constexpr int j = decltype(J)::value, blk = j >> 1;
        const char* s = src + (size_t)(blk * 32 + (i ^ ((blk & 1) << 1))) * 4096;
        DMA(s + ((j & 1) ? vo1 : vo0), L + L_V + (2 * j + lw) * 1024);
        after_issue<32 + j + 1>(cnt_addr);
      });
    }
    {
      const int r2 = lane >> 5, pos = lane & 31;
      const char* src = (const char*)wo + (size_t)(rg * 32 + 2 * lw) * 1024 + ch * 512;
      const unsigned vo0 = r2 * 1024 + ((pos ^ ((2 * lw + r2) << 1)) * 16), vo1 = r2 * 1024 + ((pos ^ (((2 * lw + r2) | 4) << 1)) * 16);
      issue_run<0, 8>([&](auto J) {
        constexpr int j = decltype(J)::value;
        DMA(src + j * 4096 + (((j >> 1) & 1) ? vo1 : vo0), L + L_WO + (2 * j + lw) * 1024);
        after_issue<48 + j + 1>(cnt_addr);
      });
      const char* srd = (const char*)wd + (size_t)(rg * 14 + 2 * lw) * 1024 + ch * 512;
      issue_run<0, 4>([&](auto J) {
        constexpr int j = decltype(J)::value;
        const int back = (j == 3) ? lw * 2048 : 0;
        DMA(srd + j * 4096 - back + ((j & 1) ? vo1 : vo0), L + L_DN + (2 * j + lw) * 1024);
        after_issue<56 + j + 1>(cnt_addr);
      });
    }
    drain_to<C_WQ>(cnt_addr); drain_to<C_K>(cnt_addr); drain_to<C_GU>(cnt_addr);
    drain_to<36>(cnt_addr); drain_to<40>(cnt_addr); drain_to<44>(cnt_addr); drain_to<48>(cnt_addr);
    drain_to<C_WO>(cnt_addr); drain_to<C_DN>(cnt_addr);
    STAMP(2);
  }
}

__global__ void k_nop() {}
constexpr int W_T = 12 << 20;
__global__ void k_pre(char* ws) {
  { const unsigned s0 = (unsigned)__builtin_amdgcn_s_memrealtime();
    while ((unsigned)__builtin_amdgcn_s_memrealtime() - s0 < PRE_SPIN) __builtin_amdgcn_s_sleep(8); }
  if (threadIdx.x == 0) *(GAS unsigned*)(ws + W_T + blockIdx.x * 4) = (unsigned)__builtin_amdgcn_s_memrealtime();
}
__global__ void k_post(char* ws) {
  const unsigned t0 = (unsigned)__builtin_amdgcn_s_memrealtime();
  unsigned best = 0xffffffffu;
  for (int k = threadIdx.x; k < 256; k += 64) { unsigned d = t0 - *(volatile GAS unsigned*)(ws + W_T + 4096 + k * 4); best = d < best ? d : best; }
  for (int o = 32; o; o >>= 1) { unsigned d = __shfl_xor(best, o); best = d < best ? d : best; }
  unsigned val = __builtin_amdgcn_readfirstlane(best);
  if (val > 5000u) val = 5000u;
  const unsigned s0 = (unsigned)__builtin_amdgcn_s_memrealtime();
  while ((unsigned)__builtin_amdgcn_s_memrealtime() - s0 < val * POST_K) __builtin_amdgcn_s_sleep(8);
}
extern "C" void kernel_launch(void* const* d_in, const int* in_sizes, int n_in, void* d_out, int out_size, void* d_ws,
                              size_t ws_size, hipStream_t stream) {
  (void)in_sizes; (void)n_in; (void)out_size; (void)ws_size;
  (void)hipFuncSetAttribute((const void*)fused_decode, hipFuncAttributeMaxDynamicSharedMemorySize, L_END + 512);
  k_pre<<<256, 64, 0, stream>>>((char*)d_ws);
  fused_decode<<<256, 512, L_END + 512, stream>>>((const u16*)d_in[0], (const u16*)d_in[1], (const u16*)d_in[2], (const u16*)d_in[3],
                                            (const u16*)d_in[4], (const u16*)d_in[6], (const u16*)d_in[7], (const u16*)d_in[8],
                                            (u16*)d_out, (char*)d_ws + WS_OFF(ws_size));
}
```

```cpp
#include <hip/hip_runtime.h>
#include <hip/hip_bf16.h>
#include <stdint.h>
#include <type_traits>

typedef unsigned short u16;
typedef unsigned long long u64;
typedef unsigned u32x4 __attribute__((ext_vector_type(4)));
typedef unsigned u32x2 __attribute__((ext_vector_type(2)));
typedef float f32x4 __attribute__((ext_vector_type(4)));
typedef float f32x2 __attribute__((ext_vector_type(2)));
typedef short s16x4 __attribute__((ext_vector_type(4)));
typedef short s16x8 __attribute__((ext_vector_type(8)));
typedef __bf16 bf16x8 __attribute__((ext_vector_type(8)));
typedef __bf16 bf16x2 __attribute__((ext_vector_type(2)));
#define LAS __attribute__((address_space(3)))
#define GAS __attribute__((address_space(1)))

constexpr int SK = 65536, WIN = 4096, DF = 1792;
constexpr int NWAVE = 14;
constexpr int THR = 9;
constexpr int CODE_KB = 12;
constexpr int QDELAY = 50, PODELAY = 40, HDELAY = 40, MLDELAY = 60;
constexpr int L_FL = 0, L_Z = 192, L_ML = 256, L_WS = 384, L_OA = 512, L_HA = 640, L_ER = 704, L_QA = 960, L_PB = L_QA + 1280,
              L_XN = L_PB + 1280, L_XR = L_XN + 1024, L_T0 = 5632;
constexpr int L_WQ = L_T0, L_GU = L_WQ + 16384, L_K = L_GU + 16384, L_V = L_K + 32768, L_WO = L_V + 32768, L_DN = L_WO + 16384,
              L_END = L_DN + 8192;
constexpr int F_CNT = L_FL, F_PF = L_FL + 16, F_POF = L_FL + 32, F_O3F = L_FL + 48, F_QPF = L_FL + 64, F_HPF = L_FL + 80,
              F_QF = L_FL + 96, F_WSF = L_FL + 104, F_OAF = L_FL + 112;
constexpr int W_E = 0, W_QC = 4096, W_QG = W_QC + 16384, W_HG = W_QG + 65536, W_ML = W_HG + 65536, W_PO = W_ML + 16384 + 4096 * 3,
              W_O3 = W_PO + (1 << 19), W_END = W_O3 + (1 << 18);
constexpr int J_WQ = 1, J_K = 10, J_V = 26, J_GU = 42, J_WO = 50, J_DN = 58, C_ALL = 62;
constexpr int C_X = 1, C_WQ = J_WQ + 8, C_K0 = J_K + 4, C_V0 = J_V + 4, C_GU = J_GU + 8, C_WO = J_WO + 8, C_DN = C_ALL;
constexpr unsigned SPIN_LDS = 1u << 24, SPIN_GLB = 1u << 20;
constexpr float LOG2E = 1.4426950408889634f;
constexpr int OOB = 0x7ffffff0;

__device__ __forceinline__ float bf_lo(unsigned w) { return __uint_as_float(w << 16); }
__device__ __forceinline__ float bf_hi(unsigned w) { return __uint_as_float(w & 0xffff0000u); }
__device__ __forceinline__ unsigned pack_bf(float lo, float hi) {
  f32x2 v = {lo, hi};
  return __builtin_bit_cast(unsigned, __builtin_convertvector(v, bf16x2));
}
__device__ __forceinline__ float rbf(float f) { return bf_lo(pack_bf(f, f)); }
__device__ __forceinline__ float rcp(float f) { return __builtin_amdgcn_rcpf(f); }
__device__ __forceinline__ float ex2(float f) { return __builtin_amdgcn_exp2f(f); }
template <int CTRL> __device__ __forceinline__ float dpp(float x) {
  return __builtin_bit_cast(float, __builtin_amdgcn_mov_dpp(__builtin_bit_cast(int, x), CTRL, 0xf, 0xf, true));
}
constexpr int XOR1 = 0xB1, XOR2 = 0x4E, ROR4 = 0x124, ROR8 = 0x128;
__device__ __forceinline__ float xrow_sum(float x) {
  auto s = __builtin_amdgcn_permlane16_swap(__float_as_uint(x), __float_as_uint(x), false, false);
  x = __uint_as_float(s[0]) + __uint_as_float(s[1]);
  auto t = __builtin_amdgcn_permlane32_swap(__float_as_uint(x), __float_as_uint(x), false, false);
  return __uint_as_float(t[0]) + __uint_as_float(t[1]);
}
__device__ __forceinline__ float row_sum(float x) {
  x += dpp<XOR1>(x); x += dpp<XOR2>(x); x += dpp<ROR4>(x); x += dpp<ROR8>(x);
  return x;
}
__device__ __forceinline__ float row_max(float x) {
  asm volatile("s_nop 1\n\t"
               "v_max_f32_dpp %0, %0, %0 quad_perm:[1,0,3,2] row_mask:0xf bank_mask:0xf\n\ts_nop 1\n\t"
               "v_max_f32_dpp %0, %0, %0 quad_perm:[2,3,0,1] row_mask:0xf bank_mask:0xf\n\ts_nop 1\n\t"
               "v_max_f32_dpp %0, %0, %0 row_ror:4 row_mask:0xf bank_mask:0xf\n\ts_nop 1\n\t"
               "v_max_f32_dpp %0, %0, %0 row_ror:8 row_mask:0xf bank_mask:0xf\n\ts_nop 1"
               : "+v"(x));
  return x;
}
__device__ __forceinline__ float vmax(float a, float b) { float r; asm("v_max_f32 %0, %1, %2" : "=v"(r) : "v"(a), "v"(b)); return r; }
__device__ __forceinline__ float wave_sum(float x) { return xrow_sum(row_sum(x)); }
__device__ __forceinline__ float sel4(int g, float a0, float a1, float a2, float a3) {
  const float lo = (g & 1) ? a1 : a0, hi = (g & 1) ? a3 : a2;
  return (g & 2) ? hi : lo;
}
__device__ __forceinline__ bool none(bool bad) { return __builtin_amdgcn_ballot_w64(bad) == 0ull; }
#define SCHED() __builtin_amdgcn_sched_barrier(0)
#define MEMBAR() asm volatile("" ::: "memory")
#define LDS_WAIT() asm volatile("s_waitcnt lgkmcnt(0)" ::: "memory")
#define RFL(x) __builtin_amdgcn_readfirstlane(x)
#define PIN(x) asm volatile("" : "+v"(x))

__device__ __forceinline__ void lds_flag(int addr, unsigned v) {
  asm volatile("ds_write_b32 %0, %1" ::"v"(addr), "v"(v) : "memory");
}
__device__ __forceinline__ unsigned red(unsigned c) { return c; }
__device__ __forceinline__ unsigned red(u32x2 c) { return min(c[0], c[1]); }
__device__ __forceinline__ unsigned red(u32x4 c) { return min(min(c[0], c[1]), min(c[2], c[3])); }
template <typename T, int SLEEP> __device__ __forceinline__ void wait_lds(LAS char* L, int off, unsigned thr) {
  __builtin_amdgcn_s_setprio(0);
#pragma nounroll
  for (unsigned n = 0; n < SPIN_LDS; ++n) {
    T raw = *(volatile LAS T*)(L + off);
    if ((unsigned)RFL(red(raw)) >= thr) break;
    if (SLEEP) __builtin_amdgcn_s_sleep(SLEEP);
  }
  __builtin_amdgcn_s_setprio(3);
  MEMBAR();
}
template <int TICKS> __device__ __forceinline__ void delay() { __builtin_amdgcn_s_sleep(TICKS * 3 / 8); }

__device__ __forceinline__ bf16x8 tr_pair(LAS char* L, int a0, int a1) {
  const s16x4 t0 = __builtin_amdgcn_ds_read_tr16_b64_v4i16((LAS s16x4*)(L + a0));
  const s16x4 t1 = __builtin_amdgcn_ds_read_tr16_b64_v4i16((LAS s16x4*)(L + a1));
  s16x8 b = {t0[0], t0[1], t0[2], t0[3], t1[0], t1[1], t1[2], t1[3]};
  return __builtin_bit_cast(bf16x8, b);
}
#define MFMA32(a, b, c) __builtin_amdgcn_mfma_f32_16x16x32_bf16((a), (b), (c), 0, 0, 0)
#define ZERO4 (f32x4{0.f, 0.f, 0.f, 0.f})
#define DMA(src, dst) __builtin_amdgcn_global_load_lds((const unsigned*)(src), (LAS unsigned*)(dst), 16, 0, 2)
#define SC1 16
#define LOAD_SC1(dst, off, rs, soff) \
  asm volatile("buffer_load_dwordx4 %0, %1, %2, %3 offen sc1" : "=v"(dst) : "v"(off), "s"(rs), "s"(soff) : "memory")
#define VM_WAIT() asm volatile("s_waitcnt vmcnt(0)" ::: "memory")

__device__ __forceinline__ unsigned piece_hash(unsigned w0, unsigned w1, unsigned w2) {
  return w0 ^ __builtin_amdgcn_alignbit(w1, w1, 21) ^ __builtin_amdgcn_alignbit(w2, w2, 10);
}
__device__ __forceinline__ unsigned piece_hash4(u32x4 w) {
  return piece_hash(w[0], w[1], w[2]) ^ __builtin_amdgcn_alignbit(w[3], w[3], 27);
}
__device__ __forceinline__ u32x4 pack4(f32x4 a, unsigned tag) {
  const unsigned D = __float_as_uint(a[3]);
  const unsigned w0 = __builtin_amdgcn_perm(__float_as_uint(a[0]), D, 0x07060501u);
  const unsigned w1 = __builtin_amdgcn_perm(__float_as_uint(a[1]), D, 0x07060502u);
  const unsigned w2 = __builtin_amdgcn_perm(__float_as_uint(a[2]), D, 0x07060503u);
  return u32x4{w0, w1, w2, piece_hash(w0, w1, w2) ^ tag};
}
__device__ __forceinline__ f32x4 unpack4(u32x4 v) {
  const unsigned t = __builtin_amdgcn_perm(v[1], v[0], 0x0c04000cu);
  return f32x4{__uint_as_float(v[0]), __uint_as_float(v[1]), __uint_as_float(v[2]),
               __uint_as_float(__builtin_amdgcn_perm(v[2], t, 0x04020100u))};
}
__device__ __forceinline__ unsigned piece_bad(u32x4 v, unsigned tag) { return piece_hash(v[0], v[1], v[2]) ^ v[3] ^ tag; }
template <int N>
__device__ __forceinline__ void gather(u32x4 rs, int off, int step, unsigned tag, u32x4 (&v)[N]) {
#pragma nounroll
  for (unsigned n = 0; n < SPIN_GLB; ++n) {
#pragma unroll
    for (int k = 0; k < N; ++k) LOAD_SC1(v[k], off, rs, k * step);
    VM_WAIT();
    unsigned bad = 0;
#pragma unroll
    for (int k = 0; k < N; ++k) { PIN(v[k]); bad |= piece_bad(v[k], tag); }
    if (none(bad != 0u)) break;
  }
}

constexpr bool is_mark(int n) {
  return n == C_X || n == C_WQ || n == C_GU || (n >= C_K0 && n <= C_K0 + 12 && ((n - C_K0) & 3) == 0) ||
         (n >= C_V0 && n <= C_V0 + 12 && ((n - C_V0) & 3) == 0) || n == C_WO || n == C_DN;
}
template <int J> __device__ __forceinline__ void after_issue(int cnt_addr) {
  if constexpr (J == THR + 1) __builtin_amdgcn_s_barrier();
  if constexpr (J > THR) {
    asm volatile("s_waitcnt vmcnt(%0)" ::"n"(THR) : "memory");
    if constexpr (is_mark(J - THR)) lds_flag(cnt_addr, J - THR);
  }
}
template <int LANDED> __device__ __forceinline__ void drain(int cnt_addr) {
  if constexpr (LANDED <= C_ALL) {
    if constexpr (is_mark(LANDED)) {
      asm volatile("s_waitcnt vmcnt(%0)" ::"n"(C_ALL - LANDED) : "memory");
      lds_flag(cnt_addr, LANDED);
    }
    drain<LANDED + 1>(cnt_addr);
  }
}
template <int J0, int N, typename F> __device__ __forceinline__ void issue_run(F f) {
  if constexpr (N > 0) {
    f(std::integral_constant<int, J0>{});
    issue_run<J0 + 1, N - 1>(f);
  }
}

__global__ void __launch_bounds__(NWAVE * 64, 4) __attribute__((amdgpu_waves_per_eu(4, 4)))
fused_decode(const u16* __restrict__ x, char* ws, const u16* __restrict__ wq, const u16* __restrict__ kc,
             const u16* __restrict__ vc, const u16* __restrict__ wg, const u16* __restrict__ wu, const u16* __restrict__ wo,
             const u16* __restrict__ wd, u16* __restrict__ out) {
  extern __shared__ __attribute__((aligned(16))) char lds_raw[];
  LAS char* L = (LAS char*)lds_raw;
  unsigned ka0, ka1;
  asm volatile("s_load_dword %0, %2, 0x0\n\ts_load_dword %1, %2, 0x40"
               : "=&s"(ka0), "=&s"(ka1) : "s"(__builtin_amdgcn_kernarg_segment_ptr()));
  const int b = blockIdx.x, tid = threadIdx.x, lane = tid & 63;
  const int wv = RFL(tid >> 6);
  u32x4 cpf;
  if (wv >= 2) {
    u64 pc0;
    asm volatile("s_getpc_b64 %0" : "=s"(pc0));
    cpf = *((const GAS u32x4*)((pc0 & ~255ull) + (size_t)min(wv - 1, CODE_KB - 1) * 1024) + lane);
  }
  const int h = b & 7, i = b >> 3;
  const int g = lane >> 4, c = lane & 15;
  const int qr = i >> 3, lu = i & 7;
  const int q_col0 = 512 * ((h + 7) & 7) + 64 * lu;
  const bool has_gu = lu != 3;
  const int u_mat = h >> 2, u_col0 = (h & 3) * 448 + (lu - (lu > 3)) * 64;
  const int ch = h >> 2;
  const int rg = ((h + (ch ? 3 : 1)) & 3) * 16 + (i >> 1), cg = 2 * ch + (i & 1);

  if (wv >= 2) {
    if (wv == 13) { *(LAS unsigned*)(L + L_FL + lane * 4) = 0u; LDS_WAIT(); }
    const __amdgpu_buffer_rsrc_t wsr = __builtin_amdgcn_make_buffer_rsrc((void*)ws, 0, W_END, 0x00020000);
    const u32x4 rs = {(unsigned)(size_t)ws, (unsigned)((size_t)ws >> 32) & 0xffffu, (unsigned)W_END, 0x00020000u};
    SCHED();
    __builtin_amdgcn_s_barrier();
    SCHED();
    asm volatile("s_waitcnt lgkmcnt(0)" ::"s"(ka0), "s"(ka1));
    asm volatile("" ::"v"(cpf));
    wait_lds<u32x2, 0>(L, F_CNT, C_X);
    const u32x4 xv = *(LAS u32x4*)(L + L_XR + lane * 16);
    const unsigned tag = RFL(*(LAS unsigned*)(L + L_ER)) * 1664525u + 1013904223u;
    const int q4 = (lane & 15) >> 2, pp = lane & 3;

    if (wv < 6) {
      const int cw = wv - 2;
      {
        float f[8];
#pragma unroll
        for (int k = 0; k < 4; ++k) { f[2 * k] = bf_lo(xv[k]); f[2 * k + 1] = bf_hi(xv[k]); }
        const float s = ((f[0] + f[1]) + (f[2] + f[3])) + ((f[4] + f[5]) + (f[6] + f[7]));
        const float mean = wave_sum(s) * (1.f / 512.f);
        float v0 = 0.f, v1 = 0.f;
#pragma unroll
        for (int k = 0; k < 8; k += 2) {
          const float d0 = f[k] - mean, d1 = f[k + 1] - mean;
          v0 = fmaf(d0, d0, v0); v1 = fmaf(d1, d1, v1);
        }
        const float var = wave_sum(v0 + v1) * (1.f / 512.f);
        const float mb = rbf(mean);
        const float den = rbf(__builtin_amdgcn_sqrtf(rbf(rbf(var) + 1e-5f)));
        const float inv = rcp(den);
        u32x4 xn;
#pragma unroll
        for (int k = 0; k < 4; ++k) {
          const unsigned d = pack_bf(f[2 * k] - mb, f[2 * k + 1] - mb);
          const float a0 = bf_lo(d), a1 = bf_hi(d);
          float q0 = a0 * inv, q1 = a1 * inv;
          q0 = fmaf(fmaf(-q0, den, a0), inv, q0); q1 = fmaf(fmaf(-q1, den, a1), inv, q1);
          xn[k] = pack_bf(q0, q1);
        }
        *(LAS u32x4*)(L + L_XN + lane * 16) = xn;
      }
      bf16x8 xa[4];
#pragma unroll
      for (int ks = 0; ks < 4; ++ks) xa[ks] = *(LAS bf16x8*)(L + L_XN + qr * 256 + g * 16 + ks * 64);
      const int tb = (8 * g + q4) * 128 + ((cw ^ (((q4 >> 1) & 1) | ((g & 1) << 1))) * 32) + pp * 8;
      auto proj = [&](int tile, unsigned thr) -> f32x4 {
        wait_lds<u32x2, 0>(L, F_CNT, thr);
        bf16x8 wb[4];
#pragma unroll
        for (int ks = 0; ks < 4; ++ks) wb[ks] = tr_pair(L, tile + tb + ks * 4096, tile + tb + ks * 4096 + 512);
        MEMBAR();
        f32x4 a = MFMA32(wb[0], xa[0], ZERO4);
        a = MFMA32(wb[1], xa[1], a); a = MFMA32(wb[2], xa[2], a); a = MFMA32(wb[3], xa[3], a);
        return a;
      };
      int qg_off = c == 0 ? W_QG + (qr * 4096 + q_col0 + 16 * cw + 4 * g) * 4 : OOB;
      int hg_off = c == 0 ? W_HG + ((u_mat * 4 + qr) * DF + u_col0 + 16 * cw + 4 * g) * 4 : OOB;
      int qc_off = c == 0 ? W_QC + (q_col0 + 16 * cw + 4 * g + qr) * 4 : OOB;
      PIN(qg_off); PIN(hg_off); PIN(qc_off);
      {
        const u32x4 a = __builtin_bit_cast(u32x4, proj(L_WQ, C_WQ));
        __builtin_amdgcn_raw_buffer_store_b128(a, wsr, qg_off, 0, SC1);
        __builtin_amdgcn_raw_buffer_store_b32(piece_hash4(a) ^ tag, wsr, qc_off, 0, SC1);
        lds_flag(F_QPF + 4 * cw, 1);
      }
      {
        bf16x8 kb[2][4], qa[4];
        wait_lds<u32x2, 1>(L, F_CNT, C_K0 + 4 * cw);
#pragma unroll
        for (int t = 0; t < 2; ++t)
#pragma unroll
          for (int ks = 0; ks < 4; ++ks)
            kb[t][ks] = *(LAS bf16x8*)(L + L_K + (2 * cw + t) * 4096 + c * 256 + (((4 * ks + g) ^ c) * 16));
        MEMBAR();
        wait_lds<u32x2, 0>(L, F_QF, 1);
#pragma unroll
        for (int ks = 0; ks < 4; ++ks) qa[ks] = *(LAS bf16x8*)(L + L_QA + (c >> 2) * 320 + ks * 64 + g * 16);
        MEMBAR();
        f32x4 s0 = MFMA32(qa[0], kb[0][0], ZERO4), s1 = MFMA32(qa[0], kb[1][0], ZERO4);
#pragma unroll
        for (int ks = 1; ks < 4; ++ks) { s0 = MFMA32(qa[ks], kb[0][ks], s0); s1 = MFMA32(qa[ks], kb[1][ks], s1); }
        unsigned r = pack_bf(s0[0], s1[0]);
        r = pack_bf(bf_lo(r) * 0.08838834764831845f, bf_hi(r) * 0.08838834764831845f);
        const float a0 = bf_lo(r), a1 = bf_hi(r);
        const float m = row_max(vmax(a0, a1));
        const float p0 = ex2((a0 - m) * LOG2E), p1 = ex2((a1 - m) * LOG2E);
        const unsigned pb = pack_bf(p0, p1);
        *(LAS u16*)(L + L_PB + g * 320 + (32 * cw + c) * 2) = (u16)pb;
        *(LAS u16*)(L + L_PB + g * 320 + (32 * cw + 16 + c) * 2) = (u16)(pb >> 16);
        const float l = row_sum(p0 + p1);
        *(LAS float*)(L + L_ML + g * 32 + cw * 4) = m;
        *(LAS float*)(L + L_ML + g * 32 + 16 + cw * 4) = l;
        lds_flag(F_PF + 4 * cw, 1);
      }
      if (has_gu) {
        wait_lds<u32x2, 1>(L, F_CNT, C_V0 + 12);
        __builtin_amdgcn_raw_buffer_store_b128(pack4(proj(L_GU, C_GU), tag), wsr, hg_off, 0, SC1);
      } else {
        wait_lds<u32x2, 1>(L, F_CNT, C_WO);
      }
      lds_flag(F_HPF + 4 * cw, 1);
      return;
    }

    if (wv < 10) {
      const int cw = wv - 6;
      int po_off = (c & 2) ? OOB : W_PO + (((4 * h + (c >> 2)) * 32 + i) * 32 + 8 * cw + 4 * (c & 1) + g) * 16;
      int o3_off = c < 4 ? W_O3 + ((32 * cg + 4 * (2 * cw + (c >> 1)) + g) * 64 + rg) * 32 + (c & 1) * 16 : OOB;
      PIN(po_off); PIN(o3_off);
      const int f = q4 | ((g & 1) << 2);
      const int tb0 = (8 * g + q4) * 256 + (((2 * cw) ^ f) * 32) + pp * 8;
      const int tb1 = (8 * g + q4) * 256 + (((2 * cw + 1) ^ f) * 32) + pp * 8;
      {
        const int pe = (c & 1) ? L_Z : L_PB + (c >> 2) * 320 + g * 16, pes = (c & 1) ? 0 : 64;
        const int pd = (c & 1) ? L_PB + (c >> 2) * 320 + g * 16 : L_Z, pds = (c & 1) ? 64 : 0;
        bf16x8 b0[4], b1[4], p0[4], p1[4];
        wait_lds<u32x2, 1>(L, F_CNT, C_V0 + 8);
#pragma unroll
        for (int ks = 0; ks < 3; ++ks) {
          b0[ks] = tr_pair(L, L_V + tb0 + ks * 8192, L_V + tb0 + ks * 8192 + 1024);
          b1[ks] = tr_pair(L, L_V + tb1 + ks * 8192, L_V + tb1 + ks * 8192 + 1024);
        }
        MEMBAR();
        wait_lds<u32x2, 1>(L, F_QF, 1);
        __builtin_amdgcn_s_setprio(0);
#pragma nounroll
        for (unsigned n = 0; n < SPIN_LDS; ++n) {
          const u32x4 pf = *(volatile LAS u32x4*)(L + F_PF);
          const u32x2 cn = *(volatile LAS u32x2*)(L + F_CNT);
          if (RFL(red(pf) != 0u && red(cn) >= (unsigned)(C_V0 + 12))) break;
        }
        __builtin_amdgcn_s_setprio(3);
        MEMBAR();
        const f32x4 m4 = *(LAS f32x4*)(L + L_ML + (c >> 2) * 32);
#pragma unroll
        for (int ks = 0; ks < 4; ++ks) {
          p0[ks] = *(LAS bf16x8*)(L + pe + ks * pes);
          p1[ks] = *(LAS bf16x8*)(L + pd + ks * pds);
        }
        b0[3] = tr_pair(L, L_V + tb0 + 3 * 8192, L_V + tb0 + 3 * 8192 + 1024);
        b1[3] = tr_pair(L, L_V + tb1 + 3 * 8192, L_V + tb1 + 3 * 8192 + 1024);
        MEMBAR();
        f32x4 t[4];
#pragma unroll
        for (int ks = 0; ks < 4; ++ks) t[ks] = MFMA32(b0[ks], p0[ks], ZERO4);
#pragma unroll
        for (int ks = 0; ks < 4; ++ks) t[ks] = MFMA32(b1[ks], p1[ks], t[ks]);
        const float M = vmax(vmax(m4[0], m4[1]), vmax(m4[2], m4[3]));
        f32x4 o = ZERO4;
#pragma unroll
        for (int ks = 0; ks < 4; ++ks) o += ex2((m4[ks] - M) * LOG2E) * t[ks];
        __builtin_amdgcn_raw_buffer_store_b128(pack4(o, tag), wsr, po_off, 0, SC1);
        lds_flag(F_POF + 4 * cw, 1);
      }
      {
        bf16x8 wb[2][2], db[2], oa[2][2], ha[2];
        wait_lds<u32x2, 1>(L, F_CNT, C_WO);
#pragma unroll
        for (int ks = 0; ks < 2; ++ks) {
          wb[0][ks] = tr_pair(L, L_WO + tb0 + ks * 8192, L_WO + tb0 + ks * 8192 + 1024);
          wb[1][ks] = tr_pair(L, L_WO + tb1 + ks * 8192, L_WO + tb1 + ks * 8192 + 1024);
        }
        MEMBAR();
        wait_lds<u32x2, 0>(L, F_CNT, C_DN);
        db[0] = tr_pair(L, L_DN + tb0, L_DN + tb0 + 1024);
        db[1] = tr_pair(L, L_DN + tb1, L_DN + tb1 + 1024);
        MEMBAR();
        const int a0 = c == 0 ? L_OA + g * 16 : L_Z, s0 = c == 0 ? 64 : 0, a1 = c == 2 ? L_OA + g * 16 : L_Z, s1 = c == 2 ? 64 : 0;
        const int h0 = c == 1 ? L_HA + g * 16 : L_Z, h1 = c == 3 ? L_HA + g * 16 : L_Z;
        wait_lds<u32x4, 0>(L, F_OAF, 1);
        oa[0][0] = *(LAS bf16x8*)(L + a0); oa[0][1] = *(LAS bf16x8*)(L + a0 + s0);
        oa[1][0] = *(LAS bf16x8*)(L + a1); oa[1][1] = *(LAS bf16x8*)(L + a1 + s1);
        ha[0] = *(LAS bf16x8*)(L + h0); ha[1] = *(LAS bf16x8*)(L + h1);
        MEMBAR();
        f32x4 r0 = MFMA32(wb[0][0], oa[0][0], ZERO4), r1 = MFMA32(wb[1][0], oa[1][0], ZERO4);
        r0 = MFMA32(wb[0][1], oa[0][1], r0); r1 = MFMA32(wb[1][1], oa[1][1], r1);
        r0 = MFMA32(db[0], ha[0], r0); r1 = MFMA32(db[1], ha[1], r1);
        __builtin_amdgcn_raw_buffer_store_b128(pack4(r0 + r1, tag), wsr, o3_off, 0, SC1);
        lds_flag(F_O3F + 4 * cw, 1);
      }
      return;
    }

    const int gw = wv - 10;
    const int r = rg >> 1, dh = rg & 1;
    if (gw < 2) {
      {
        wait_lds<u32x4, 1>(L, F_QPF, 1);
        delay<QDELAY>();
        u32x4 v[5];
        const int off = ((4 * h + 2 * gw) * 128 + 4 * lane) * 4;
#pragma nounroll
        for (unsigned n = 0; n < SPIN_GLB; ++n) {
#pragma unroll
          for (int k = 0; k < 5; ++k) LOAD_SC1(v[k], off, rs, W_QC + k * 16384);
          VM_WAIT();
          unsigned bad = 0;
          PIN(v[0]);
#pragma unroll
          for (int k = 1; k < 5; ++k) { PIN(v[k]); bad |= piece_hash4(v[k]) ^ v[0][k - 1] ^ tag; }
          if (none(bad != 0u)) break;
          __builtin_amdgcn_s_sleep(1);
        }
        const f32x4 q = (__builtin_bit_cast(f32x4, v[1]) + __builtin_bit_cast(f32x4, v[2])) +
                        (__builtin_bit_cast(f32x4, v[3]) + __builtin_bit_cast(f32x4, v[4]));
        *(LAS u32x2*)(L + L_QA + (2 * gw + (lane >> 5)) * 320 + (lane & 31) * 8) = u32x2{pack_bf(q[0], q[1]), pack_bf(q[2], q[3])};
        lds_flag(F_QF + 4 * gw, 1);
      }
      {
        wait_lds<u32x4, 1>(L, F_POF, 1);
        delay<PODELAY>();
        u32x4 v[4];
        f32x4 w;
        const int off = W_PO + ((r * 32 + (lane >> 3)) * 32 + 16 * dh + 8 * gw + (lane & 7)) * 16;
#pragma nounroll
        for (unsigned n = 0; n < SPIN_GLB; ++n) {
#pragma unroll
          for (int k = 0; k < 4; ++k) LOAD_SC1(v[k], off, rs, k * 4096);
          VM_WAIT();
          const unsigned wsf = *(volatile LAS unsigned*)(L + F_WSF);
          MEMBAR();
          w = *(LAS f32x4*)(L + L_WS + (lane >> 3) * 16);
          MEMBAR();
          unsigned bad = 0;
#pragma unroll
          for (int k = 0; k < 4; ++k) { PIN(v[k]); bad |= piece_bad(v[k], tag); }
          PIN(bad);
          if (none(bad != 0u || wsf == 0u)) break;
        }
        f32x4 a = (w[0] * unpack4(v[0]) + w[1] * unpack4(v[1])) + (w[2] * unpack4(v[2]) + w[3] * unpack4(v[3]));
#pragma unroll
        for (int k = 0; k < 4; ++k) a[k] = xrow_sum(a[k] + dpp<ROR8>(a[k]));
        *(LAS u32x2*)(L + L_OA + gw * 64 + (lane & 7) * 8) = u32x2{pack_bf(a[0], a[1]), pack_bf(a[2], a[3])};
        lds_flag(F_OAF + 4 * gw, 1);
      }
      if (gw == 0 && (i & 1) == 0) {
        const int cgrp = 64 * (1 - ch) + (h & 3) * 16 + (i >> 1);
        const int j = (lane >> 1) & 3;
        const unsigned xw = *(LAS unsigned*)(L + L_XR + cgrp * 8 + (j >> 1) * 4);
        const float xe = (j & 1) ? bf_hi(xw) : bf_lo(xw);
        wait_lds<u32x4, 1>(L, F_O3F, 1);
        u32x4 v[2];
        gather<2>(rs, W_O3 + cgrp * 2048 + lane * 16, 1024, tag, v);
        f32x4 a = unpack4(v[0]) + unpack4(v[1]);
#pragma unroll
        for (int k = 0; k < 4; ++k) { a[k] += dpp<XOR2>(a[k]); a[k] += dpp<ROR4>(a[k]); }
        float y = sel4(j, a[0], a[1], a[2], a[3]);
        y = rbf(xrow_sum(y + dpp<ROR8>(y)));
        const float d = dpp<XOR1>(y);
        const float gt = rbf(d * rcp(1.f + ex2(-d * LOG2E)));
        const unsigned res = pack_bf(xe + rbf(y * gt), 0.f);
        if (lane < 8 && (lane & 1) == 0) *(GAS u16*)(out + cgrp * 4 + j) = (u16)res;
        if (b == 0 && lane == 0) __hip_atomic_store((GAS unsigned*)(ws + W_E), tag, __ATOMIC_RELAXED, __HIP_MEMORY_SCOPE_AGENT);
      }
    } else if (gw == 2) {
      int ml_off = c == 0 ? W_ML + ((4 * h + g) * 32 + i) * 16 : OOB;
      PIN(ml_off);
      wait_lds<u32x2, 1>(L, F_QF, 1);
      wait_lds<u32x4, 0>(L, F_PF, 1);
      {
        const f32x4 m4 = *(LAS f32x4*)(L + L_ML + g * 32), l4 = *(LAS f32x4*)(L + L_ML + g * 32 + 16);
        const float M = vmax(vmax(m4[0], m4[1]), vmax(m4[2], m4[3]));
        const float Ls = (ex2((m4[0] - M) * LOG2E) * l4[0] + ex2((m4[1] - M) * LOG2E) * l4[1]) +
                         (ex2((m4[2] - M) * LOG2E) * l4[2] + ex2((m4[3] - M) * LOG2E) * l4[3]);
        const u32x4 ml = {__float_as_uint(M), tag, __float_as_uint(Ls), tag};
        __builtin_amdgcn_raw_buffer_store_b128(ml, wsr, ml_off, 0, SC1);
      }
      delay<MLDELAY>();
      u32x4 v;
      const int off = W_ML + (r * 32 + (lane & 31)) * 16;
#pragma nounroll
      for (unsigned n = 0; n < SPIN_GLB; ++n) {
        LOAD_SC1(v, off, rs, 0);
        VM_WAIT();
        PIN(v);
        if (none(((v[1] ^ tag) | (v[3] ^ tag)) != 0u)) break;
        __builtin_amdgcn_s_sleep(1);
      }
      const float m = __uint_as_float(v[0]), l = __uint_as_float(v[2]);
      float M = row_max(m);
      auto s = __builtin_amdgcn_permlane16_swap(__float_as_uint(M), __float_as_uint(M), false, false);
      M = vmax(__uint_as_float(s[0]), __uint_as_float(s[1]));
      const float e = ex2((m - M) * LOG2E);
      float Ls = row_sum(e * l);
      auto t = __builtin_amdgcn_permlane16_swap(__float_as_uint(Ls), __float_as_uint(Ls), false, false);
      Ls = __uint_as_float(t[0]) + __uint_as_float(t[1]);
      *(LAS float*)(L + L_WS + ((lane & 7) * 4 + ((lane & 31) >> 3)) * 4) = e * rcp(Ls);
      lds_flag(F_WSF, 1);
    } else {
      wait_lds<u32x4, 1>(L, F_HPF, 1);
      delay<HDELAY>();
      u32x4 v[1];
      gather<1>(rs, W_HG + ((lane >> 3) * DF + rg * 28 + 4 * min(lane & 7, 6)) * 4, 0, tag, v);
      f32x4 a = unpack4(v[0]);
#pragma unroll
      for (int k = 0; k < 4; ++k) a[k] += dpp<ROR8>(a[k]);
      const int j = (lane >> 3) & 3;
      const float ya = (lane & 8) ? a[1] : a[0], yb = (lane & 8) ? a[3] : a[2];
      auto s = __builtin_amdgcn_permlane16_swap(__float_as_uint(ya), __float_as_uint(yb), false, false);
      const float y = __uint_as_float(s[0]) + __uint_as_float(s[1]);
      auto t = __builtin_amdgcn_permlane32_swap(__float_as_uint(y), __float_as_uint(y), false, false);
      const unsigned gu = pack_bf(__uint_as_float(t[0]), __uint_as_float(t[1]));
      const float gg = bf_lo(gu), uu = bf_hi(gu);
      const float z = 0.7978845608028654f * (gg + 0.044715f * gg * gg * gg);
      const float ge = rbf(gg * rcp(1.f + ex2(-2.f * LOG2E * z)));
      const unsigned hb = (lane & 7) < 7 ? pack_bf(ge * uu, 0.f) : 0u;
      *(LAS u16*)(L + L_HA + (4 * (lane & 7) + j) * 2) = (u16)hb;
      asm volatile("ds_write_b64 %0, %1" ::"v"(F_OAF + 8), "v"(u32x2{1u, 1u}) : "memory");
    }
    return;
  }

  {
    const int lw = wv;
    const int cnt_addr = F_CNT + 4 * lw;
    __builtin_amdgcn_s_setprio(2);
    const size_t hoff = ((size_t)h * SK + (SK - WIN)) * 256;
    const int prow = lane >> 3, pchunk = (lane & 7) ^ ((((prow >> 1) & 1) | (lw << 1)) << 1);
    unsigned vq = (qr * 128 + lw * 8 + prow) * 8192 + q_col0 * 2 + pchunk * 16;
    PIN(vq);
    asm volatile("s_waitcnt lgkmcnt(0)" ::"s"(ka0), "s"(ka1), "s"(x), "s"(ws), "s"(wq), "s"(kc));
    if (lw == 0) __builtin_amdgcn_global_load_lds((const unsigned*)(x + lane * 8), (LAS unsigned*)(L + L_XR), 16, 0, 0);
    else __builtin_amdgcn_global_load_lds((const unsigned*)(ws + W_E), (LAS unsigned*)(L + L_ER), 4, 0, SC1);
    issue_run<0, 8>([&](auto J) {
      constexpr int j = decltype(J)::value;
      DMA((const char*)wq + (size_t)j * 16 * 8192 + vq, L + L_WQ + (2 * j + lw) * 1024);
      after_issue<J_WQ + j + 1>(cnt_addr);
    });
    {
      const int k4 = lane & 3;
      const char* p = k4 == 0 ? (const char*)vc + hoff + (size_t)(lw * 128 + i) * 4096
                    : k4 == 1 ? (const char*)(u_mat ? wu : wg) + (size_t)(qr * 128 + lw * 127) * (DF * 2) + u_col0 * 2
                    : k4 == 2 ? (const char*)wo + (size_t)(rg * 64 + lw * 63) * 1024 + cg * 256
                              : (const char*)wd + (size_t)(rg * 28 + lw * 27) * 1024 + cg * 256;
      if (has_gu) p = (const char*)kc + hoff + (size_t)i * 4096 + lw * 1024;
      __builtin_amdgcn_global_load_lds((const unsigned*)p, (LAS unsigned*)(L + L_DN + lw * 1024), 4, 0, 2);
      after_issue<J_K>(cnt_addr);
    }
    {
      const char* src = (const char*)kc + hoff + lw * 1024;
      const unsigned vo0 = g * 256 + ((c ^ (4 * lw + g)) * 16), vo1 = g * 256 + ((c ^ (4 * (lw + 2) + g)) * 16) + 2048;
      issue_run<0, 16>([&](auto J) {
        constexpr int j = decltype(J)::value, blk = j >> 1;
        const char* s = src + (size_t)(blk * 32 + (i ^ ((blk & 1) << 1))) * 4096;
        DMA(s + ((j & 1) ? vo1 : vo0), L + L_K + (2 * j + lw) * 1024);
        after_issue<J_K + j + 1>(cnt_addr);
      });
    }
    {
      const char* src = (const char*)vc + hoff + lw * 1024;
      const unsigned vo0 = g * 256 + ((c ^ (g << 1)) * 16), vo1 = g * 256 + ((c ^ ((g | 4) << 1)) * 16) + 2048;
      issue_run<0, 16>([&](auto J) {
        constexpr int j = decltype(J)::value, blk = j >> 1;
        const char* s = src + (size_t)(blk * 32 + (i ^ ((blk & 1) << 1))) * 4096;
        DMA(s + ((j & 1) ? vo1 : vo0), L + L_V + (2 * j + lw) * 1024);
        after_issue<J_V + j + 1>(cnt_addr);
      });
    }
    {
      const char* src = has_gu ? (const char*)(u_mat ? wu : wg) + (size_t)(qr * 128 + lw * 8) * (DF * 2) + u_col0 * 2
                               : (const char*)wo + (size_t)(rg * 64 + 4 * lw) * 1024 + cg * 256;
      const unsigned vu = has_gu ? prow * (DF * 2) + pchunk * 16 : 0;
      const unsigned step = has_gu ? 16 * (DF * 2) : 8192;
      issue_run<0, 8>([&](auto J) {
        constexpr int j = decltype(J)::value;
        DMA(src + (size_t)(j * step) + vu, L + L_GU + (2 * j + lw) * 1024);
        after_issue<J_GU + j + 1>(cnt_addr);
      });
    }
    {
      const unsigned vo0 = g * 1024 + ((c ^ (g << 1)) * 16), vo1 = g * 1024 + ((c ^ ((g | 4) << 1)) * 16);
      const char* src = (const char*)wo + (size_t)(rg * 64 + 4 * lw) * 1024 + cg * 256;
      issue_run<0, 8>([&](auto J) {
        constexpr int j = decltype(J)::value;
        DMA(src + j * 8192 + ((j & 1) ? vo1 : vo0), L + L_WO + (2 * j + lw) * 1024);
        after_issue<J_WO + j + 1>(cnt_addr);
      });
      const char* srd = (const char*)wd + (size_t)(rg * 28 + 4 * lw) * 1024 + cg * 256;
      issue_run<0, 4>([&](auto J) {
        constexpr int j = decltype(J)::value;
        const int back = (j == 3) ? lw * 4096 : 0;
        DMA(srd + j * 8192 - back + ((j & 1) ? vo1 : vo0), L + L_DN + (2 * j + lw) * 1024);
        after_issue<J_DN + j + 1>(cnt_addr);
      });
    }
    drain<C_ALL - THR + 1>(cnt_addr);
  }
}

extern "C" void kernel_launch(void* const* d_in, const int* in_sizes, int n_in, void* d_out, int out_size, void* d_ws,
                              size_t ws_size, hipStream_t stream) {
  (void)in_sizes; (void)n_in; (void)out_size; (void)ws_size;
  (void)hipFuncSetAttribute((const void*)fused_decode, hipFuncAttributeMaxDynamicSharedMemorySize, L_END);
  fused_decode<<<256, NWAVE * 64, L_END, stream>>>((const u16*)d_in[0], (char*)d_ws, (const u16*)d_in[3], (const u16*)d_in[1],
                                                   (const u16*)d_in[2], (const u16*)d_in[6], (const u16*)d_in[7],
                                                   (const u16*)d_in[4], (const u16*)d_in[8], (u16*)d_out);
}
```
